```python
import torch

def _softmax(x, axis=-1):
    return torch.softmax(x, dim=axis)
def _gelu(x):
    return torch.nn.functional.gelu(x, approximate="tanh")
def _silu(x):
    return torch.nn.functional.silu(x)
def _sigmoid(x):
    return torch.sigmoid(x)
def _layernorm(x, axis=-1, eps=1e-5):
    m = x.mean(dim=axis, keepdim=True)
    v = x.var(dim=axis, keepdim=True, unbiased=False)
    return (x - m) / torch.sqrt(v + eps)
def _scatter_add(tgt, idx, src, axis=0):
    return tgt.clone().index_add_(axis, idx, src)
def _segment_sum(src, seg, n):
    out = torch.zeros((n,) + src.shape[1:], dtype=src.dtype)
    return out.index_add_(0, seg, src)
def _pad(x, pw):
    flat = tuple(v for pair in reversed(pw) for v in pair)
    return torch.nn.functional.pad(x, flat)

kernel_name = 'concat_heads__sliding_block_cached__compressed_kv_cla__decode'

def setup_inputs():

    _w = (lambda a: (lambda t: t.to(torch.bfloat16) if t.is_floating_point() else t)(torch.as_tensor(a)))
    def _A_setup():
        B, Sq, D = 4, 1, 512
        Hq, Hkv, Sk, Dh = 32, 32, 4096, 128
        assert Hq % Hkv == 0, (Hq, Hkv)
        return {
            "x": _w((lambda *s: torch.randn(*s, dtype=torch.bfloat16) * 0.8)(B, Sq, D)),
            "k_cache": _w((lambda *s: torch.randn(*s, dtype=torch.bfloat16) * 0.8)(B, Hkv, Sk, Dh)),
            "v_cache": _w((lambda *s: torch.randn(*s, dtype=torch.bfloat16) * 0.05)(B, Hkv, Sk, Dh)),
            "wq": _w((lambda *s: torch.randn(*s, dtype=torch.bfloat16) * 0.8)(D, Hq * Dh) * (D ** -0.5)),
            "wo": _w((lambda *s: torch.randn(*s, dtype=torch.bfloat16) * 0.05)(Hq * Dh, D) * ((Hq * Dh) ** -0.5)),
        }
    _A_in = _A_setup()

    def _B_setup():
        B, H, Sq, Sk, D, M = 4, 32, 1, 4096, 128, 512
        Dc = 4 * D
        return {
            "x": _w((lambda *s: torch.randn(*s, dtype=torch.bfloat16) * 1.6)(B, Sq, M)),
            "c_kv": _w((lambda *s: torch.randn(*s, dtype=torch.bfloat16) * 0.1)(B, Sk, Dc)),
            "w_q": _w((lambda *s: torch.randn(*s, dtype=torch.bfloat16) * 3.2)(M, H, D) * (M ** -0.5)),
            "w_uk": _w((lambda *s: torch.randn(*s, dtype=torch.bfloat16) * 3.2)(Dc, H, D) * (Dc ** -0.5)),
            "w_uv": _w((lambda *s: torch.randn(*s, dtype=torch.bfloat16) * 0.1)(Dc, H, D) * (Dc ** -0.5)),
            "w_o": _w((lambda *s: torch.randn(*s, dtype=torch.bfloat16) * 0.1)(H, D, M) * ((H * D) ** -0.5)),
        }
    _B_in = _B_setup()

    return {
        "A_x": _A_in["x"],
        "A_k_cache": _A_in["k_cache"],
        "A_v_cache": _A_in["v_cache"],
        "A_wq": _A_in["wq"],
        "A_wo": _A_in["wo"],
        "B_x": _B_in["x"],
        "B_c_kv": _B_in["c_kv"],
        "B_w_q": _B_in["w_q"],
        "B_w_uk": _B_in["w_uk"],
        "B_w_uv": _B_in["w_uv"],
        "B_w_o": _B_in["w_o"],
    }

def reference(A_x, A_k_cache, A_v_cache, A_wq, A_wo, B_x, B_c_kv, B_w_q, B_w_uk, B_w_uv, B_w_o):
    def _A_ref(x, k_cache, v_cache, wq, wo):
        B, Sq, D = x.shape
        Hq, Dh = 32, 128
        Hkv, Sk = k_cache.shape[1], k_cache.shape[2]
        W = min(4096, max(256, Sk // 4))
        rep = Hq // Hkv
        q = (lambda x, *d: x.permute(*d))((x @ wq).reshape(B, Sq, Hq, Dh), 0, 2, 1, 3)
        k = (lambda x, n, axis: torch.repeat_interleave(x, n, dim=axis))(k_cache[:, :, Sk - W:, :], rep, axis=1)
        v = (lambda x, n, axis: torch.repeat_interleave(x, n, dim=axis))(v_cache[:, :, Sk - W:, :], rep, axis=1)
        scores = torch.einsum("bhqd,bhkd->bhqk", q, k) * (Dh ** -0.5)
        p = _softmax(scores, axis=-1)
        o = torch.einsum("bhqk,bhkd->bhqd", p, v)
        return (lambda x, *d: x.permute(*d))(o, 0, 2, 1, 3).reshape(B, Sq, Hq * Dh) @ wo

    def _B_ref(x, c_kv, w_q, w_uk, w_uv, w_o):
        B, Sq, M = x.shape[0], x.shape[1], x.shape[2]
        Sk = c_kv.shape[1]
        H, D = w_q.shape[1], w_q.shape[2]
        q = torch.einsum("bqm,mhd->bhqd", x, w_q)
        k = torch.einsum("bkc,chd->bhkd", c_kv, w_uk)
        v = torch.einsum("bkc,chd->bhkd", c_kv, w_uv)
        scores = torch.einsum("bhqd,bhkd->bhqk", q, k) * (D ** -0.5)
        off = Sk - Sq
        qi = torch.arange(Sq)[:, None]
        kj = torch.arange(Sk)[None, :]
        cmask = kj <= (qi + off)
        scores = torch.where(cmask[None, None], scores, -1e9)
        p = _softmax(scores, axis=-1)
        ctx = torch.einsum("bhqk,bhkd->bhqd", p, v)
        return torch.einsum("bhqd,hdm->bqm", ctx, w_o)

    a = _A_ref(A_x, A_k_cache, A_v_cache, A_wq, A_wo)
    b = _B_ref(B_x, B_c_kv, B_w_q, B_w_uk, B_w_uv, B_w_o)
    return torch.concatenate([a, b], axis=-1)
```

```cpp
#define PROLE 0
#define PIDX 0
#define PB -1
#pragma clang diagnostic ignored "-Wint-to-pointer-cast"
#include <hip/hip_runtime.h>
#include <hip/hip_bf16.h>
#include <stdint.h>
#include <utility>

typedef unsigned int u32;
typedef unsigned short u16;
typedef unsigned long long u64;
using u32x4  = __attribute__((ext_vector_type(4))) unsigned;
using u32x2  = __attribute__((ext_vector_type(2))) unsigned;
using f32x4  = __attribute__((ext_vector_type(4))) float;
using bf16x8 = __attribute__((ext_vector_type(8))) short;
using s16x4  = __attribute__((ext_vector_type(4))) short;
#define DI __device__ __forceinline__
#define LDSP __attribute__((address_space(3)))
#define SCHED_FENCE() __builtin_amdgcn_sched_barrier(0)
#define PIN(x) asm volatile("" : "+v"(x))

DI float bflo(u32 v) { return __uint_as_float(v << 16); }
DI float bfhi(u32 v) { return __uint_as_float(v & 0xffff0000u); }
using f32x2_ = __attribute__((ext_vector_type(2))) float;
using bf16x2_ = __attribute__((ext_vector_type(2))) __bf16;
DI u32 pk2(float lo, float hi) { return __builtin_bit_cast(u32, __builtin_convertvector(f32x2_{lo, hi}, bf16x2_)); }
template <int CTRL> DI float dppf(float v) { return __builtin_bit_cast(float, __builtin_amdgcn_update_dpp(0, __builtin_bit_cast(int, v), CTRL, 0xf, 0xf, true)); }
template <int CTRL> DI u32 dppu(u32 v) { return (u32)__builtin_amdgcn_update_dpp(0, (int)v, CTRL, 0xf, 0xf, true); }
DI float row16_sum(float v) { v += dppf<0x128>(v); v += dppf<0x124>(v); v += dppf<0x122>(v); v += dppf<0x121>(v); return v; }
DI float row16_max(float v) { v = fmaxf(v, dppf<0x128>(v)); v = fmaxf(v, dppf<0x124>(v)); v = fmaxf(v, dppf<0x122>(v)); v = fmaxf(v, dppf<0x121>(v)); return v; }
DI float row16_maxq(float v) {
  asm volatile("s_nop 1\n\tv_max_f32_dpp %0, %0, %0 row_ror:8 row_mask:0xf bank_mask:0xf\n\ts_nop 1\n\tv_max_f32_dpp %0, %0, %0 row_ror:4 row_mask:0xf bank_mask:0xf\n\t"
               "s_nop 1\n\tv_max_f32_dpp %0, %0, %0 row_ror:2 row_mask:0xf bank_mask:0xf\n\ts_nop 1\n\tv_max_f32_dpp %0, %0, %0 row_ror:1 row_mask:0xf bank_mask:0xf\n\ts_nop 1" : "+v"(v));
  return v;
}
DI float row16_sumq(float v) {
  asm volatile("s_nop 1\n\tv_add_f32_dpp %0, %0, %0 row_ror:8 row_mask:0xf bank_mask:0xf\n\ts_nop 1\n\tv_add_f32_dpp %0, %0, %0 row_ror:4 row_mask:0xf bank_mask:0xf\n\t"
               "s_nop 1\n\tv_add_f32_dpp %0, %0, %0 row_ror:2 row_mask:0xf bank_mask:0xf\n\ts_nop 1\n\tv_add_f32_dpp %0, %0, %0 row_ror:1 row_mask:0xf bank_mask:0xf\n\ts_nop 1" : "+v"(v));
  return v;
}
DI float max3q(float a, float b, float c) { float d; asm("v_max3_f32 %0, %1, %2, %3" : "=v"(d) : "v"(a), "v"(b), "v"(c)); return d; }
DI float max8q(f32x4 a, f32x4 b) { return max3q(max3q(a[0], a[1], a[2]), max3q(a[3], b[0], b[1]), max3q(b[2], b[3], b[3])); }
DI float x64_maxq(float v) {
  auto s = __builtin_amdgcn_permlane16_swap(__float_as_uint(v), __float_as_uint(v), false, false);
  v = max3q(__uint_as_float(s[0]), __uint_as_float(s[1]), __uint_as_float(s[1]));
  auto t = __builtin_amdgcn_permlane32_swap(__float_as_uint(v), __float_as_uint(v), false, false);
  return max3q(__uint_as_float(t[0]), __uint_as_float(t[1]), __uint_as_float(t[1]));
}
DI float x16_sum(float v) { auto s = __builtin_amdgcn_permlane16_swap(__float_as_uint(v), __float_as_uint(v), false, false); return __uint_as_float(s[0]) + __uint_as_float(s[1]); }
DI float x32_sum(float v) { auto s = __builtin_amdgcn_permlane32_swap(__float_as_uint(v), __float_as_uint(v), false, false); return __uint_as_float(s[0]) + __uint_as_float(s[1]); }
DI float x16_max(float v) { auto s = __builtin_amdgcn_permlane16_swap(__float_as_uint(v), __float_as_uint(v), false, false); return fmaxf(__uint_as_float(s[0]), __uint_as_float(s[1])); }
DI float x32_max(float v) { auto s = __builtin_amdgcn_permlane32_swap(__float_as_uint(v), __float_as_uint(v), false, false); return fmaxf(__uint_as_float(s[0]), __uint_as_float(s[1])); }
using bf16x2 = __attribute__((ext_vector_type(2))) __bf16;
DI float dot2(u32 a, u32 b, float c) { return __builtin_amdgcn_fdot2_f32_bf16(__builtin_bit_cast(bf16x2, a), __builtin_bit_cast(bf16x2, b), c, false); }
DI u32 plo(u32 r1, u32 r0) { return __builtin_amdgcn_perm(r1, r0, 0x05040100u); }
DI u32 phi(u32 r1, u32 r0) { return __builtin_amdgcn_perm(r1, r0, 0x07060302u); }
DI bf16x8 bfrag(const u32x4* R, int j) {
  const int q = j >> 1;
  u32x4 o;
  if (j & 1) { o[0] = phi(R[1][q], R[0][q]); o[1] = phi(R[3][q], R[2][q]); o[2] = phi(R[5][q], R[4][q]); o[3] = phi(R[7][q], R[6][q]); }
  else       { o[0] = plo(R[1][q], R[0][q]); o[1] = plo(R[3][q], R[2][q]); o[2] = plo(R[5][q], R[4][q]); o[3] = plo(R[7][q], R[6][q]); }
  return __builtin_bit_cast(bf16x8, o);
}
DI s16x4 kfrag(const u32x4* R, int j) {
  const int q = j >> 1;
  u32x2 o;
  if (j & 1) { o[0] = phi(R[1][q], R[0][q]); o[1] = phi(R[3][q], R[2][q]); }
  else       { o[0] = plo(R[1][q], R[0][q]); o[1] = plo(R[3][q], R[2][q]); }
  return __builtin_bit_cast(s16x4, o);
}
DI f32x4 sel4(f32x4 a, f32x4 b, f32x4 c, f32x4 d, int r) { f32x4 lo = (r & 1) ? b : a, hi = (r & 1) ? d : c; return (r & 2) ? hi : lo; }
DI f32x4 mfma16(bf16x8 a, bf16x8 b, f32x4 c) { return __builtin_amdgcn_mfma_f32_16x16x32_bf16(a, b, c, 0, 0, 0); }
DI bf16x8 asbf(u32x4 v) { return __builtin_bit_cast(bf16x8, v); }
DI float dot8(const u32x4 kv, const u32x4 q) {
  float s = dot2(kv[0], q[0], 0.f);
  s = dot2(kv[1], q[1], s); s = dot2(kv[2], q[2], s); s = dot2(kv[3], q[3], s);
  return s;
}
constexpr int NB = 4, NH = 32, DH = 128, SK = 4096, WIN = 1024, DM = 512, DC = 512;
constexpr float LOG2E = 1.4426950408889634f;
constexpr float SCL2 = 0.088388347648318440f * LOG2E;

#define WOFF 8
#define AW 3
#define ASYNC 2
#define A0 4
#define PX 14
#define PY 22
#define CU 2
#define FLI 10
#define FLW 0
#define UVP 3
#define OWP 8
#define CPF 3
#define CPN 1
#define CPL 191
#define ND 3
#define THC 325
#define PT0 900
#define PD1 270
#define PN1 12
#define PD2 248
#define PN2 38
#define BT0 900
#define BD1 440
#define BDQ 60
#define BD2 390
#define XR 4
#define UKR 5
#define X0 8
#define TI 3
#define T2N 1
#define TH 13
#define T4N 1
#define WLG 3
#define CK1 8
#define QA 3
#define TFS 4
#define WK0 17
#define WKD 7
#define NOA 0
#define SIBS 0, 0, 0, 0, 4, 4, 4, 4, 8, 8, 8, 8, 8, 8, 8, 8
constexpr int sibs_[17] = {SIBS, 16};
constexpr int sb(int i) { return i < 0 ? 0 : sibs_[i]; }
constexpr int C0 = 16 - 2 * ND;
constexpr int NCH = 16;
constexpr u32 WS_EPOCH = 0;
constexpr u32 WS_QP    = 4096;
constexpr u32 PBLK     = 4608;
constexpr u32 WS_PART  = WS_QP + NB * 8 * 384 * 16;
constexpr u32 WS_QX    = WS_PART + NB * 16 * 8 * PBLK;
constexpr u32 WS_ACC   = WS_QX + NH * NB * 4 * DH * 8;
constexpr u32 WS_ACX   = WS_ACC + 2 * 65536;
constexpr u32 WS_FLG   = WS_ACX + 32 * 4096;
constexpr u32 WS_XA    = WS_FLG + 128 * 128;
constexpr u32 WS_XB    = WS_XA + 32 * 2048 * 16;
constexpr u32 WS_XC    = WS_XB + 32 * 2048 * 16;
constexpr u32 WS_END   = WS_XC + 32 * 2048 * 16;
constexpr u32 WS_TS    = WS_END;
constexpr u32 RSRC_W3  = 0x00020000;
constexpr u32 SPIN_CAP = 1u << 17;

constexpr int L_T    = 0;
constexpr int L_Q    = 131072;
constexpr int L_P    = L_Q + 16384;
constexpr int L_MX   = L_P + 8192;
constexpr int L_UKT  = L_Q;
constexpr int L_S1P  = L_Q + 4096;
constexpr int L_QB   = L_Q;
constexpr int L_QO   = L_Q + 2048;
constexpr int L_LAT  = L_Q;
constexpr int L_RS   = L_LAT + 2304;
constexpr int L_EX   = L_Q + 16384;
constexpr int L_ENDB = 163840;
constexpr int L_AP   = 0;
constexpr int L_AQ   = L_AP + 4096;
constexpr int L_AST  = L_AQ + 2048;
constexpr int L_AR2  = L_AST + 4352;
constexpr int L_AX   = L_AR2 + 16384;
constexpr int L_END  = L_ENDB;

DI u32 fsw(u32 key) { return (key & 2) | ((key >> 1) & 4); }
DI int ppos(int r, int c16) { return 32 * r + 2 * c16; }
DI int pcol(int p) { return 8 * ((p >> 1) & 15) + 2 * (p >> 5) + (p & 1); }
struct KArgs {
  const u16* xa; const u16* kc; const u16* vc; const u16* wqa; const u16* woa;
  const u16* xb; const u16* ckv; const u16* wqb; const u16* wuk; const u16* wuv; const u16* wob;
  char* ws; u16* out;
};

DI u32x4 ldnt(const u16* p) { return __builtin_nontemporal_load(reinterpret_cast<const u32x4*>(p)); }
DI u32x4 ldsh(const u16* p) { return *reinterpret_cast<const u32x4*>(p); }
DI u32x4 ld_sc1(__amdgpu_buffer_rsrc_t rs, u32 off) { return __builtin_amdgcn_raw_buffer_load_b128(rs, off, 0, 16); }
DI u32x2 ld_sc1_64(__amdgpu_buffer_rsrc_t rs, u32 off) { return __builtin_amdgcn_raw_buffer_load_b64(rs, off, 0, 16); }
DI void st_sc1(__amdgpu_buffer_rsrc_t rs, u32 off, u32x4 v) { __builtin_amdgcn_raw_buffer_store_b128(v, rs, off, 0, 16); }
DI void st_sc1_32(__amdgpu_buffer_rsrc_t rs, u32 off, u32 v) { __builtin_amdgcn_raw_buffer_store_b32(v, rs, off, 0, 16); }
DI void wg_barrier() { asm volatile("s_waitcnt lgkmcnt(0)\n\ts_barrier" ::: "memory"); }

#define TS(i) do { if (PROLE == ROLE_ && (i) == PIDX) tsx_ = __builtin_amdgcn_s_memrealtime(); } while (0)
#define TSRAW
#define TSARG , u64& tsx_, const int ROLE_
#define TSPASS , tsx_, ROLE_
#define TS_DECL u64 tsx_ = 0
#define TS_FLUSH() do { const u64 d_ = (tsx_ && (PB < 0 || (blockIdx.x >> 6) == PB)) ? tsx_ - ts0_ : 0; while (__builtin_amdgcn_s_memrealtime() - ts0_ < 4000 + d_) __builtin_amdgcn_s_sleep(2); } while (0)
DI u64 fx_add(char* ws, __amdgpu_buffer_rsrc_t rs, float y, u32 aoff, u32 xown, u32 tag) {
  const float t = __builtin_fabsf(y);
  u64 x = __builtin_bit_cast(u64, __builtin_fma((double)y, 0x1p40, 0x1.8p52)) + (0x0400000000000000ull - 0x4338000000000000ull);
  PIN(x);
  const u64 big = __ballot(!(t < 64.f)), some = __ballot(t >= 0x1p-24f);
  u64 odd;
  asm("s_cmp_eq_u64 %2, 0\n\ts_cselect_b64 %0, -1, %1" : "=s"(odd) : "s"(big), "s"(some) : "scc");
  if (__builtin_expect(odd != 0ull, 0)) {
    if (__ballot(t != 0.f) != 0ull) {
      st_sc1(rs, xown, u32x4{__float_as_uint(y), tag, ~__float_as_uint(y), tag});
      x = 0x0410000000000000ull;
      asm volatile("s_waitcnt vmcnt(0)" ::: "memory");
    }
  }
  return __hip_atomic_fetch_add(reinterpret_cast<u64*>(ws + aoff), x, __ATOMIC_RELAXED, __HIP_MEMORY_SCOPE_AGENT) + x;
}
template <int NARR, int XSTR>
DI float fx_val(__amdgpu_buffer_rsrc_t rs, u64 t, u32 x0, u32 tag) {
  float s = (float)__builtin_fma(__builtin_bit_cast(double, (t & 0xfffffffffffffull) | 0x4330000000000000ull), 0x1p-40, -0x1.8p12);
  const u32 np = (u32)(t >> 52) & 63u;
  if (__builtin_expect(np != 0u, 0)) {
    float e = 0.f;
#pragma unroll 1
    for (u32 spins = 0; spins < 64; ++spins) {
      u32 c = 0; e = 0.f;
#pragma unroll 4
      for (u32 p = 0; p < NARR; ++p) {
        const u32x4 v = ld_sc1(rs, x0 + p * XSTR);
        const bool ok = (v[1] == tag) & (v[3] == tag) & (v[0] == ~v[2]);
        e += ok ? __uint_as_float(v[0]) : 0.f; c += ok;
      }
      if (c == np) break;
    }
    s += e;
  }
  return s;
}
template <int CTRL, int BANK> DI u32 dppb(u32 old, u32 v) { return (u32)__builtin_amdgcn_update_dpp((int)old, (int)v, CTRL, 0xf, BANK, false); }
constexpr u32 OOB = 0x80000000u;
template <int N>
DI void row_issue(__amdgpu_buffer_rsrc_t rs, const u32 (&off)[N], u32x4 (&v)[N]) {
#pragma unroll
  for (int p = 0; p < N; ++p) v[p] = ld_sc1(rs, off[p]);
}
template <int N>
DI void row_wait(__amdgpu_buffer_rsrc_t rs, const u32 (&off)[N], u32x4 (&v)[N], u32 tag, bool act, int lane) {
  bool ok = true;
#pragma unroll
  for (int p = 0; p < N; ++p) PIN(v[p]);
#pragma unroll
  for (int p = 0; p < N; ++p) ok &= (v[p][3] == tag);
  if (__builtin_expect(!__all(ok | !act), 0)) {
#pragma unroll 1
    for (u32 spins = 0; spins < SPIN_CAP; ++spins) {
      __builtin_amdgcn_s_sleep(2);
      asm volatile("" ::: "memory");
      u32x4 t[N]; bool re[N];
#pragma unroll
      for (int p = 0; p < N; ++p) {
        const u64 m = __ballot(act & !((v[p][3] == tag)));
        re[p] = ((u32)(m >> (lane & 48)) & 0xffffu) != 0u;
        t[p] = ld_sc1(rs, re[p] ? off[p] : OOB);
      }
      bool k2 = true;
#pragma unroll
      for (int p = 0; p < N; ++p) { v[p] = re[p] ? t[p] : v[p]; PIN(v[p]); k2 &= (v[p][3] == tag); }
      if (__all(k2 | !act)) break;
    }
  }
}
template <int NS> DI void pace(u32 t0) {
  SCHED_FENCE();
  if (NS > 0)
    asm volatile("s_branch .Lpc%=\n.Lpl%=:\n\ts_sleep 1\n.Lpc%=:\n\ts_memrealtime s[98:99]\n\ts_waitcnt lgkmcnt(0)\n\ts_sub_u32 s98, s98, %0\n\ts_cmpk_lt_u32 s98, %1\n\ts_cbranch_scc1 .Lpl%="
                 :: "s"(t0), "n"(NS / 10) : "s98", "s99", "scc", "memory");
  SCHED_FENCE();
}
template <class F, int... I> DI void unroll_i(F f, std::integer_sequence<int, I...>) { (f(std::integral_constant<int, I>{}), ...); }
template <int N, class F> DI void unroll_c(F f) { unroll_i(f, std::make_integer_sequence<int, N>{}); }
#define IC(x) std::integral_constant<int, x>{}
#define INL __attribute__((always_inline))
DI int lane_id() { int l; asm volatile("v_mbcnt_lo_u32_b32 %0, -1, 0\n\tv_mbcnt_hi_u32_b32 %0, -1, %0" : "=v"(l)); return l; }
DI u32 clk32() { return (u32)__builtin_amdgcn_s_memrealtime(); }
DI void hint_wait(__amdgpu_buffer_rsrc_t rs, u32 off, u32x2 hv, u32 tag, int dt) {
  PIN(hv);
  if (__builtin_expect(!__all(hv[1] == tag), 0)) {
#pragma unroll 1
    for (u32 spins = 0; spins < 8; ++spins) {
      asm volatile("" ::: "memory");
      const u32x2 t = ld_sc1_64(rs, hv[1] != tag ? off : OOB);
      hv = hv[1] != tag ? t : hv;
      PIN(hv);
      if (__all(hv[1] == tag)) break;
    }
  }
  const u32 t0 = clk32();
  const float d = row16_maxq(hv[1] == tag ? (float)(int)(hv[0] - t0) : -1000.f);
  const int n = min(__builtin_amdgcn_readfirstlane((int)d) + dt, 250);
#pragma unroll 1
  for (int i = 0; i < 256 && (int)(clk32() - t0) < n; ++i) __builtin_amdgcn_s_sleep(1);
  asm volatile("" ::: "memory");
}
DI void both_halves(__amdgpu_buffer_rsrc_t rs, u32 off, u32x4& hv, u32 tag) {
  PIN(hv);
  u32 m = (u32)__ballot((hv[0] == tag) & (hv[1] == tag) & (hv[3] == tag));
  if (__builtin_expect(!(m & 0xffu) | !(m & 0xff00u), 0)) {
#pragma unroll 1
    for (u32 spins = 0; spins < SPIN_CAP; ++spins) {
      __builtin_amdgcn_s_sleep(2);
      asm volatile("" ::: "memory");
      hv = ld_sc1(rs, off);
      PIN(hv);
      m = (u32)__ballot((hv[0] == tag) & (hv[1] == tag) & (hv[3] == tag));
      if ((m & 0xffu) && (m & 0xff00u)) break;
    }
  }
}
DI u32 next_tag(u32 e) { return e * 1664525u + 1013904223u; }

DI void s1_mfma(const u32x4 (&R)[2][8], const u32x4 (&xf)[2], int r, f32x4& s0, f32x4& s1) {
  f32x4 acc[8];
#pragma unroll
  for (int j = 0; j < 8; ++j) acc[j] = mfma16(asbf(xf[0]), bfrag(R[0], j), f32x4{0.f, 0.f, 0.f, 0.f});
#pragma unroll
  for (int j = 0; j < 8; ++j) PIN(acc[j]);
  SCHED_FENCE();
#pragma unroll
  for (int j = 0; j < 8; ++j) acc[j] = mfma16(asbf(xf[1]), bfrag(R[1], j), acc[j]);
  s0 = sel4(acc[0], acc[2], acc[4], acc[6], r); s1 = sel4(acc[1], acc[3], acc[5], acc[7], r);
}
DI int qoffs(int qd, int rot) { const int aq = (qd + rot) & 3; return 32 * (aq >> 1) + 4 * (aq & 1); }
DI void x_load(const u16* xsrc, int rot, u32x4 (&xf)[2]) {
#pragma unroll
  for (int k = 0; k < 2; ++k) {
    const u32x2 lo = *reinterpret_cast<const u32x2*>(xsrc + qoffs(2 * k, rot)), hi = *reinterpret_cast<const u32x2*>(xsrc + qoffs(2 * k + 1, rot));
    xf[k] = u32x4{lo[0], lo[1], hi[0], hi[1]};
  }
}
constexpr int trow_t(int i) { return i < XR ? BT0 + i * BD1 : i < X0 ? BT0 + (i + 4) * BD1 : BT0 + (X0 + 4) * BD1 + 12 * BDQ + (i - X0) * BD2; }
constexpr int wq_t(int n) { return n < 4 ? BT0 + (XR + n) * BD1 : BT0 + (X0 + 4) * BD1 + (n - 4) * BDQ; }
template <int LO, int HI> DI void s1_load(const u16* wsrc, int rot, u32x4 (&R)[2][8], u32 t0) {
  unroll_c<4 * (HI - LO)>([&](auto k) {
    constexpr int n = 4 * LO + k, qd = n >> 2, i = n & 3;
    if (i == 0 && qd) { SCHED_FENCE(); __builtin_amdgcn_s_barrier(); }
    pace<wq_t(n)>(t0);
    R[qd >> 1][4 * (qd & 1) + i] = ldsh(wsrc + (size_t)(qoffs(qd, rot) + i) * (NH * DH));
  });
}

DI float score4(const u32x4 (&k)[4], const u32x4& qpk, bool hi8, bool hi4) {
  const float d0 = dot8(k[0], qpk), d1 = dot8(k[1], qpk), d2 = dot8(k[2], qpk), d3 = dot8(k[3], qpk);
  const float a0 = (hi8 ? d2 : d0) + dppf<0x128>(hi8 ? d0 : d2), a1 = (hi8 ? d3 : d1) + dppf<0x128>(hi8 ? d1 : d3);
  float s = (hi4 ? a1 : a0) + dppf<0x141>(hi4 ? a0 : a1);
  s += dppf<0xB1>(s); s += dppf<0x4E>(s);
  return s * SCL2;
}
DI s16x4 ppack(float p) { return __builtin_bit_cast(s16x4, u32x2{pk2(p, dppf<0x12c>(p)), pk2(dppf<0x128>(p), dppf<0x124>(p))}); }
DI void q_finish(__amdgpu_buffer_rsrc_t rs, u32 off, u32x4 v, u32 tag, char* dst, int lane) {
  PIN(v);
  if (__builtin_expect(!__all((v[1] == tag) & (v[3] == tag)), 0)) {
#pragma unroll 1
    for (u32 spins = 0; spins < SPIN_CAP; ++spins) {
      asm volatile("" ::: "memory");
      v = ld_sc1(rs, off);
      PIN(v);
      if (__all((v[1] == tag) & (v[3] == tag))) break;
      __builtin_amdgcn_s_sleep(1);
    }
  }
  float q0 = __uint_as_float(v[0]), q1 = __uint_as_float(v[2]);
  q0 += dppf<0x128>(q0); q1 += dppf<0x128>(q1);
  q0 = x16_sum(q0); q1 = x16_sum(q1);
  if (lane < 8) *reinterpret_cast<u32*>(dst) = pk2(q0, q1);
}

DI void roleB(const KArgs& a, char* smem, const int g, const int idx, const int w, const int lane0, const u32x4 (&uk)[4], const u32 tvo, const u32 cpf, const u32 t0, const u64 traw, const u64 ts0_) {
  constexpr int ROLE_ = 2;
  TS_DECL; TSRAW;
  const int h = 4 * (idx & 7) + ((idx >> 3) & 3), e4 = idx >> 5;
  const int b3 = idx & 3, kp = 2 * (idx >> 4) + ((idx >> 2) & 1), hh = (idx >> 3) & 1;
  const __amdgpu_buffer_rsrc_t rs = __builtin_amdgcn_make_buffer_rsrc((void*)a.ws, 0, (int)WS_END, RSRC_W3);
  const __amdgpu_buffer_rsrc_t rt = __builtin_amdgcn_make_buffer_rsrc((void*)a.ckv, 0, NB * SK * DC * 2, RSRC_W3);
  u32 tag = *reinterpret_cast<const u32*>(a.ws + WS_EPOCH);
  u32x4 tv[32], uv[4], ow[4];
  auto wload = [&](int pos) INL {
    if (pos == UVP) {
      const int l = lane_id();
      const char* p = reinterpret_cast<const char*>(a.wuv + ((size_t)(128 * e4 + 16 * w) * NH + h) * DH);
      const u32 o = (l >> 4) * (8 * NH * DH) + (l & 15) * 16;
#pragma unroll
      for (int n = 0; n < 4; ++n) uv[n] = ldnt(reinterpret_cast<const u16*>(p + n * (NH * DH * 2) + o));
    }
    if (pos == OWP) {
      const int l = lane_id();
      const char* p = reinterpret_cast<const char*>(a.wob + (size_t)(h * DH + 16 * w) * DM + 128 * e4);
      const u32 o = (l >> 4) * (8 * DM) + (l & 15) * 16;
#pragma unroll
      for (int n = 0; n < 4; ++n) ow[n] = ldnt(reinterpret_cast<const u16*>(p + n * (DM * 2) + o));
    }
  };
  const u32 tvl = tvo & ~(8u << 17), tso = (u32)hh << 20, tro = (idx >> 4) + (idx & 3);
  auto cluster = [&](auto i) INL {
    unroll_c<sb(i) - sb(i - 1)>([&](auto k0) INL { constexpr int k = sb(i - 1) + k0; tv[2 * (k & 8) + 8 + (k & 7)] = __builtin_amdgcn_raw_buffer_load_b128(rt, tvl, (tso ^ (1u << 20)) + ((k + tro) & 15) * 8192, 0); });
  };
  auto tload = [&](auto lo, auto hi) INL {
    unroll_c<hi - lo>([&](auto i0) INL {
      constexpr int i = lo + i0;
      pace<trow_t(i)>(t0);
      if constexpr (i != X0) cluster(IC(i));
      tv[2 * (i & 8) + (i & 7)] = __builtin_amdgcn_raw_buffer_load_b128(rt, tvl, tso + ((i + tro) & 15) * 8192, 0);
      SCHED_FENCE();
    });
  };
  if (w >= 6) st_sc1(rs, WS_ACC + (u32)((idx * 2 + w - 6) * 64 + lane0) * 16, u32x4{0u, 0x80000u, 0u, 0x80000u});
  SCHED_FENCE();
  constexpr int QN = 3;
  u32x4 qv[QN]; u32 qoff, pa64[QN], pa32[QN], ack, atf[2], atw;
  {
    const int lane = lane0, r = lane >> 4, c16 = lane & 15;
#pragma unroll
    for (int k = 0; k < QN; ++k) {
      const int G = 64 * k + lane, hl = 8 * (w & 1) + G / 24, rec = G % 24, u = rec / 3, j = rec - 3 * u, sc = 16 * (w >> 1) + 2 * u;
      pa64[k] = L_Q + hl * 1024 + (((sc + (j >> 1)) ^ hl) << 4) + 8 * (j & 1);
      pa32[k] = L_Q + hl * 1024 + (((sc + 1) ^ hl) << 4) + (j == 2 ? 4 : 8 + 4 * j);
      PIN(pa64[k]); PIN(pa32[k]);
    }
    ack = L_T + (16 * w + c16) * 128 + ((r ^ fsw(c16)) << 4);
    const u32 tq = c16 >> 2, tp = lane & 3;
#pragma unroll
    for (int u = 0; u < 2; ++u) { const u32 rr = 8 * r + tq + 4 * u; atf[u] = L_T + w * 16384 + rr * 128 + (((tp >> 1) ^ fsw(rr)) << 4) + 8 * (tp & 1); PIN(atf[u]); }
    atw = L_T + (lane >> 3) * 16384 + w * 128 + (((lane & 7) ^ fsw(w)) << 4);
    PIN(ack); PIN(atw);
  }
  auto twrite = [&](int half, int lo, int hi) INL {
    SCHED_FENCE();
    u32 aw[2] = {atw, atw};
    PIN(aw[1]);
    aw[1] ^= 64u;
#pragma unroll
    for (int i = 0; i < 17; ++i) {
      if (i < lo || i >= hi) continue;
#pragma unroll
      for (int k = 0; k < 16; ++k)
        if (k >= sb(i - 1) && k < sb(i) && (k >> 3) == half) *reinterpret_cast<LDSP u32x4*>((LDSP char*)aw[k & 1] + (8 + (k & 7)) * 1024) = tv[2 * (k & 8) + 8 + (k & 7)];
      if (i < 16 && (i >> 3) == half) *reinterpret_cast<LDSP u32x4*>((LDSP char*)aw[i & 1] + (i & 7) * 1024) = tv[2 * (i & 8) + (i & 7)];
    }
    SCHED_FENCE();
  };
  SCHED_FENCE();
  {
    const int lane = lane0, tid = w * 64 + lane, r = lane >> 4, c16 = lane & 15;
    u32x4 ua[4];
    const int m0 = 64 * w + 8 * r;
    const u16* wsrc = a.wqb + (size_t)m0 * (NH * DH) + h * DH + 8 * c16;
    u32x4 R[2][8], xf[2];
    static_assert(X0 >= 8 && TH >= X0 + TI + T2N && sb(X0) >= 8);
    x_load(a.xb + (c16 & 3) * DM + m0, e4, xf);
    unroll_c<X0 + 1>([&](auto i) INL {
      if constexpr (i == UKR) {
        SCHED_FENCE();
        asm volatile("" :: "v"(cpf));
        tag = next_tag(__builtin_amdgcn_readfirstlane(tag));
        char* us = smem + L_UKT + w * 4096;
#pragma unroll
        for (int k = 0; k < 4; ++k) *reinterpret_cast<u32x4*>(us + (4 * r + k) * 256 + ((c16 ^ (4 * r + k)) << 4)) = uk[k];
        asm volatile("" ::: "memory");
#pragma unroll
        for (int k = 0; k < 4; ++k) ua[k] = *reinterpret_cast<const u32x4*>(us + c16 * 256 + (((4 * k + r) ^ c16) << 4));
#pragma unroll
        for (int k = 0; k < 4; ++k) PIN(ua[k]);
      }
      if constexpr (i == XR) s1_load<0, 1>(wsrc, e4, R, t0);
      if constexpr (i < X0) tload(i, IC(i + 1));
      if constexpr (i < X0 && i >= WLG) twrite(0, i - WLG, i - WLG + 1);
    });
    pace<wq_t(4)>(t0);
    cluster(IC(X0));
    s1_load<1, 4>(wsrc, e4, R, t0);
    SCHED_FENCE();
    tload(IC(X0), IC(X0 + TI));
    twrite(0, X0 - WLG, X0 + 1);
    f32x4 s0, s1;
    s1_mfma(R, xf, r, s0, s1);
    float* pw = reinterpret_cast<float*>(smem + L_S1P) + w * 512 + ppos(r, c16);
#pragma unroll
    for (int b = 0; b < NB; ++b) *reinterpret_cast<u32x2*>(pw + b * DH) = u32x2{__float_as_uint(s0[b]), __float_as_uint(s1[b])};
    SCHED_FENCE();
    tload(IC(X0 + TI), IC(X0 + TI + T2N));
    wg_barrier();
    SCHED_FENCE();
    tload(IC(X0 + TI + T2N), IC(TH));
    const float* pp = reinterpret_cast<const float*>(smem + L_S1P) + tid;
    const float s = ((pp[0] + pp[512]) + (pp[1024] + pp[1536])) + ((pp[2048] + pp[2560]) + (pp[3072] + pp[3584]));
    *reinterpret_cast<u16*>(smem + L_QB + (tid >> 7) * 288 + pcol(tid & 127) * 2) = (u16)pk2(s, s);
    wg_barrier();
    SCHED_FENCE();
    tload(IC(TH), IC(TH + T4N));
    f32x4 d0 = f32x4{0.f, 0.f, 0.f, 0.f};
#pragma unroll
    for (int i = 0; i < 4; ++i) {
      const bf16x8 qf = *reinterpret_cast<const bf16x8*>(smem + L_QB + (c16 & 3) * 288 + (32 * i + 8 * r) * 2);
      d0 = mfma16(asbf(ua[i]), qf, d0);
    }
    if (c16 < 4) {
      char* o = smem + L_QO + w * 256 + c16 * 48;
      *reinterpret_cast<u32*>(o + (r < 3 ? 16 * r : 8)) = pk2(d0[0], d0[1]);
      *reinterpret_cast<u32*>(o + (r < 3 ? 16 * r + 4 : 24)) = pk2(d0[2], d0[3]);
    }
    wg_barrier();
    if (w < 4 && lane < 24) {
      u32x4 x = *reinterpret_cast<const u32x4*>(smem + L_QO + (lane / 3) * 256 + (3 * w + lane % 3) * 16);
      x[3] = tag; x[2] = lane % 3 == 2 ? x[1] : x[2];
      st_sc1(rs, WS_QP + (u32)(((w * 4 + e4) * 32 + h) * 24 + lane) * 16, x);
    }
  }
  SCHED_FENCE();
  TS(2);
  u32x4 ck[2][16];
  u32 ac[4], at[4][2];
  auto ckaddr = [&]() INL { PIN(ack); ac[0] = ack; ac[1] = ack ^ 64u; ac[2] = ac[0] + 65536u; ac[3] = ac[1] + 65536u; };
  auto tfaddr = [&]() INL {
#pragma unroll
    for (int u = 0; u < 2; ++u) {
      PIN(atf[u]);
#pragma unroll
      for (int ct = 0; ct < 4; ++ct) at[ct][u] = atf[u] ^ (u32)(ct << 5);
    }
  };
  auto ckread = [&](int kb, int s) INL { ck[kb][s] = *reinterpret_cast<const LDSP u32x4*>((const LDSP char*)ac[2 * (s >> 3) + (s & 1)] + ((s >> 1) & 3) * 16384); };
  bf16x8 tf[2][8][2];
  auto tfread = [&](int kb, int k2) INL {
#pragma unroll
    for (int cb = 0; cb < 2; ++cb) {
      s16x4 b0 = __builtin_amdgcn_ds_read_tr16_b64_v4i16((LDSP s16x4*)((LDSP char*)at[2 * (k2 & 1) + cb][0] + 4096 * (k2 >> 1)));
      s16x4 b1 = __builtin_amdgcn_ds_read_tr16_b64_v4i16((LDSP s16x4*)((LDSP char*)at[2 * (k2 & 1) + cb][1] + 4096 * (k2 >> 1)));
      tf[kb][k2][cb] = __builtin_shufflevector(b0, b1, 0, 1, 2, 3, 4, 5, 6, 7);
    }
  };
  constexpr int R0 = TH + T4N, R1 = (R0 + 17) / 2;
  tload(IC(R0), IC(R1));
  tfaddr();
#pragma unroll
  for (int k2 = 0; k2 < 8; ++k2) tfread(0, k2);
  SCHED_FENCE();
  tload(IC(R1), IC(16));
  ckaddr();
#pragma unroll
  for (int s = 0; s < 16; ++s) ckread(0, s);
  SCHED_FENCE();
  wg_barrier();
#pragma unroll
  for (int s = 0; s < 16; ++s) { PIN(ck[0][s]); PIN(tf[0][s >> 1][s & 1]); }
  SCHED_FENCE();
  TS(13);
  cluster(IC(16));
  twrite(1, 0, 17);
  qoff = WS_QP + (u32)((b3 * 8 + (w & 6) + hh) * 384 + 192 * (w & 1) + lane_id()) * 16;
#pragma unroll
  for (int k = 0; k < QN; ++k) qv[k] = ld_sc1(rs, qoff + 1024 * k);
  SCHED_FENCE();
  TS(14);
  wg_barrier();
  auto so = [](int n) { return (n >> 2) + 4 * (n & 3); };
  ckaddr();
#pragma unroll
  for (int n = 0; n < CK1; ++n) ckread(1, so(n));
  u32 aq, amw, hco;
  {
    const int lane = lane_id(), r = lane >> 4, c16 = lane & 15;
    aq = L_Q + c16 * 1024 + ((r ^ c16) << 4);
    amw = L_MX + (w >> 2) * 256 + c16 * 16 + (w & 3) * 4;
    PIN(aq); PIN(amw);
    SCHED_FENCE();
    TS(15);
    const u32 qo[QN] = {qoff, qoff + 1024, qoff + 2048};
    row_wait<QN>(rs, qo, qv, tag, true, lane);
    SCHED_FENCE(); TS(16); SCHED_FENCE();
#pragma unroll
    for (int k = 0; k < QN; ++k) {
      *reinterpret_cast<LDSP u32x2*>((LDSP char*)pa64[k]) = u32x2{qv[k][0], qv[k][1]};
      *reinterpret_cast<LDSP u32*>((LDSP char*)pa32[k]) = qv[k][2];
    }
    SCHED_FENCE();
    wload(3);
    SCHED_FENCE();
    wg_barrier();
    SCHED_FENCE();
    TS(17);
  }
  u32x4 hc;
  SCHED_FENCE();
  {
    const u32 tsc = clk32();
    f32x4 sc[2], sd[2];
    u32x4 qa[16];
    u32 amx, apw, amr, apf[4], gb, gm, lane, r, c16;
    auto qread = [&](int n) INL { qa[n] = *reinterpret_cast<const LDSP u32x4*>((const LDSP char*)(aq ^ (u32)(64 * (n >> 2))) + (n & 3) * 256); };
#pragma unroll
    for (int n = 0; n < QA; ++n) qread(n);
#pragma unroll
    for (int n = 0; n < 16; ++n) {
      SCHED_FENCE();
      if (n + QA < 16) qread(n + QA);
      if (n + CK1 < 16) ckread(1, so(n + CK1));
      SCHED_FENCE();
#pragma unroll
      for (int kb = 0; kb < 2; ++kb) {
        if (n & 1) sd[kb] = mfma16(asbf(ck[kb][so(n)]), asbf(qa[n]), n > 1 ? sd[kb] : f32x4{0.f, 0.f, 0.f, 0.f});
        else sc[kb] = mfma16(asbf(ck[kb][so(n)]), asbf(qa[n]), n ? sc[kb] : f32x4{0.f, 0.f, 0.f, 0.f});
      }
      if (n == 0) st_sc1(rs, w == 7 && lane_id() == 0 ? WS_FLG + (u32)idx * 128 : OOB, u32x4{tag, tag, tsc, tag});
      if (n == 8) {
        lane = lane_id(); r = lane >> 4; c16 = lane & 15;
        amx = L_MX + c16 * 16;
        apw = L_P + c16 * 512 + (((2 * w + (r >> 1)) ^ c16) << 4) + 8 * (r & 1);
        PIN(amx); PIN(apw);
      }
      if (n == 9) { amr = L_MX + (c16 & 4) * 128 + (4 * r + (c16 & 2) + (c16 >> 3)) * 16; PIN(amr); }
      if (n == 10) {
        gb = WS_PART + (u32)((b3 * 16 + kp) * 8 + w) * PBLK; gm = gb + 4096 + hh * 256 + lane * 4;
        gb += hh * 2048 + r * 512 + c16 * 16;
        hco = WS_FLG + (u32)((w & 3) + 4 * (c16 & 1) + (c16 & 8) + 16 * ((w & 4) + ((c16 & 7) >> 1))) * 128;
        PIN(gb); PIN(gm); PIN(hco);
      }
      if (n == 11) {
#pragma unroll
        for (int k = 0; k < 4; ++k) { apf[k] = (L_P + c16 * 512 + ((r ^ c16) << 4)) ^ (u32)(k << 6); PIN(apf[k]); }
      }
      if (n == 12) {
        tfaddr();
#pragma unroll
        for (int k = 0; k < 8; ++k) PIN(at[k >> 1][k & 1]);
      }
    }
    SCHED_FENCE();
    {
      sc[0] += sd[0]; sc[1] += sd[1];
      *reinterpret_cast<LDSP float*>((LDSP char*)amw) = x64_maxq(max8q(sc[0], sc[1])) * SCL2;
    }
    SCHED_FENCE(); TS(4); SCHED_FENCE();
    wload(4);
    SCHED_FENCE();
    wg_barrier();
    SCHED_FENCE();
    {
      const f32x4 a0 = *reinterpret_cast<const LDSP f32x4*>((const LDSP char*)amx), a1 = *reinterpret_cast<const LDSP f32x4*>((const LDSP char*)amx + 256);
      SCHED_FENCE();
#pragma unroll
      for (int k2 = 0; k2 < TFS; ++k2) tfread(1, k2);
      SCHED_FENCE();
      const float m = max8q(a0, a1);
      const f32x4 x0 = sc[0], x1 = sc[1];
      u32x2 p0, p1;
      p0[0] = pk2(__builtin_amdgcn_exp2f(x0[0] * SCL2 - m), __builtin_amdgcn_exp2f(x0[1] * SCL2 - m));
      p0[1] = pk2(__builtin_amdgcn_exp2f(x0[2] * SCL2 - m), __builtin_amdgcn_exp2f(x0[3] * SCL2 - m));
      p1[0] = pk2(__builtin_amdgcn_exp2f(x1[0] * SCL2 - m), __builtin_amdgcn_exp2f(x1[1] * SCL2 - m));
      p1[1] = pk2(__builtin_amdgcn_exp2f(x1[2] * SCL2 - m), __builtin_amdgcn_exp2f(x1[3] * SCL2 - m));
      *reinterpret_cast<LDSP u32x2*>((LDSP char*)apw) = p0;
      *reinterpret_cast<LDSP u32x2*>((LDSP char*)apw + 256) = p1;
      SCHED_FENCE();
      const float ls = x32_sum(x16_sum(dot2(p0[1], 0x3f803f80u, dot2(p0[0], 0x3f803f80u, 0.f)) + dot2(p1[1], 0x3f803f80u, dot2(p1[0], 0x3f803f80u, 0.f))));
      *reinterpret_cast<LDSP float*>((LDSP char*)amw + 512) = ls;
    }
    wg_barrier();
    SCHED_FENCE(); TS(5); SCHED_FENCE();
    hc = ld_sc1(rs, hco);
    SCHED_FENCE();
    u32x4 pf[8];
    f32x4 m0, m1;
    auto pread = [&](int k) INL { pf[k] = *reinterpret_cast<const LDSP u32x4*>((const LDSP char*)apf[k & 3] + (k >> 2) * 256); };
#pragma unroll
    for (int k = 0; k < 4; ++k) { pread(k); SCHED_FENCE(); }
    wload(5);
    SCHED_FENCE();
    f32x4 acc[4];
    u32 cs[2], mw;
    auto pack = [&](int ip) INL {
      const u32 a0 = pk2(acc[0][2 * ip], acc[1][2 * ip]), a1 = pk2(acc[2][2 * ip], acc[3][2 * ip]);
      const u32 b0 = pk2(acc[0][2 * ip + 1], acc[1][2 * ip + 1]), b1 = pk2(acc[2][2 * ip + 1], acc[3][2 * ip + 1]);
      const u32x4 x = u32x4{dppb<0x128, 0xc>(a0, b0) + tag, dppb<0x128, 0xc>(a1, b1), dppb<0x128, 0x3>(b0, a0), dppb<0x128, 0x3>(b1, a1)};
      st_sc1(rs, gb + ip * 256, x);
      u32 c = (x[0] + x[1]) + (x[2] + x[3]);
      c += dppu<0xB1>(c); c += dppu<0x4E>(c);
      cs[ip] = c;
      PIN(cs[ip]);
    };
#pragma unroll
    for (int n = 0; n < 32; ++n) {
      acc[n & 3] = mfma16(asbf(pf[n >> 2]), tf[n >> 4][2 * ((n >> 2) & 3) + ((n >> 1) & 1)][n & 1], n < 4 ? f32x4{0.f, 0.f, 0.f, 0.f} : acc[n & 3]);
      SCHED_FENCE();
      if (n % 3 == 0 && n / 3 < 4) pread(4 + n / 3);
      if (n % 2 == 1 && TFS + n / 2 < 8) tfread(1, TFS + n / 2);
      if (n == 18) { m0 = *reinterpret_cast<const LDSP f32x4*>((const LDSP char*)amr); m1 = *reinterpret_cast<const LDSP f32x4*>((const LDSP char*)amr + 256); }
      if (n == 27) {
        PIN(m0); PIN(m1);
        const float mx = max8q(m0, m1);
        const float sm = ((m0[0] + m0[1]) + (m0[2] + m0[3])) + ((m1[0] + m1[1]) + (m1[2] + m1[3]));
        mw = __float_as_uint((c16 & 4) ? sm : mx);
        PIN(mw);
      }
      SCHED_FENCE();
    }
    SCHED_FENCE(); TS(6); SCHED_FENCE();
    pack(0); pack(1);
    st_sc1_32(rs, gm, mw + ((c16 & 1) ? ((c16 & 2) ? cs[1] : cs[0]) + tag : 0u));
    SCHED_FENCE();
  }
  const int lane = lane_id(), tid = w * 64 + lane, r = lane >> 4, c16 = lane & 15;
  s16x4 owf[8]; bf16x8 uvf[4];
  auto uvwrite = [&]() {
#pragma unroll
    for (int n = 0; n < 4; ++n) {
      const int c = 16 * w + 4 * r + n;
      *reinterpret_cast<u32x4*>(smem + L_T + w * 16384 + (4 * r + n) * 256 + ((c16 ^ (2 * (((c >> 4) & 3) | ((c & 1) << 2)))) << 4)) = uv[n];
    }
  };
  auto uvread = [&]() {
    const u32 tq = c16 >> 2, tp = lane & 3;
#pragma unroll
    for (int s = 0; s < 4; ++s) {
      const u32 c0 = 64 * (s >> 1) + 16 * tq + 4 * (s & 1) + r, sw = 2 * (tq | (4 * (r & 1)));
      const char* p = smem + L_T + (c0 >> 4) * 16384 + (c0 & 15) * 256 + (((2 * w + (tp >> 1)) ^ sw) << 4) + 8 * (tp & 1);
      s16x4 b0 = __builtin_amdgcn_ds_read_tr16_b64_v4i16((LDSP s16x4*)p);
      s16x4 b1 = __builtin_amdgcn_ds_read_tr16_b64_v4i16((LDSP s16x4*)(p + 8 * 256));
      uvf[s] = __builtin_shufflevector(b0, b1, 0, 1, 2, 3, 4, 5, 6, 7);
      if (UVP < 6) PIN(uvf[s]);
    }
  };
  auto owpack = [&]() {
#pragma unroll
    for (int j = 0; j < 8; ++j) { owf[j] = kfrag(ow, j); PIN(owf[j]); }
  };
  if (UVP < 6) uvwrite();
  wg_barrier();
  SCHED_FENCE();
  TS(20);
  wload(6);
  SCHED_FENCE();
  const u32 pblk = WS_PART + (u32)(((w & 3) * 16 + 8 * (w >> 2) + r) * 8 + 2 * e4 + (c16 >> 3)) * PBLK;
  u32 poff = pblk + h * 128 + ((c16 & 7) << 4), moff = pblk + (c16 & 1) * (32 * PBLK) + 4096 + (4 * (h >> 2) + 2 * (h & 1) + ((c16 >> 2) & 1)) * 16 + 4 * (h & 2);
  PIN(moff);
  u32 lrd = L_LAT + (c16 & 3) * 288 + 16 * r, mrd = L_RS + 16 * (c16 & 3);
  const u32 word = (tid >> 7) * 512 + 128 * e4 + (tid & 127);
  u32 cxa = WS_ACX + (u32)(h * 512 + tid) * 8, exw = L_EX + (w * 512 + ppos(r, c16)) * 4;
  u32 aoff = WS_ACC + 65536 + (word >> 3) * 256 + (word & 7) * 8, ooff = ((tid >> 7) * 1024 + 512 + 128 * e4 + pcol(tid & 127)) * 2;
  {
    u32 off[2]; u32x4 v[2]; u32x2 mv;
#pragma unroll
    for (int t = 0; t < 2; ++t) { off[t] = poff + (u32)(t * 32 * PBLK); PIN(off[t]); }
    both_halves(rs, hco, hc, tag);
    if (idx == 0 && w == 7) {
      const u32 eoff = WS_QX + (u32)((lane >> 2) * 16 + (lane & 3)) * DH * 8;
#pragma unroll 1
      for (u32 spins = 0; spins < SPIN_CAP; ++spins) {
        const u32x4 e0 = ld_sc1(rs, eoff), e1 = ld_sc1(rs, eoff + 16 * 16 * DH * 8);
        if (__all((e0[1] == tag) & (e0[3] == tag) & (e1[1] == tag) & (e1[3] == tag))) break;
        asm volatile("" ::: "memory");
      }
      if (lane == 0) st_sc1_32(rs, WS_EPOCH, tag);
    }
    hint_wait(rs, hco + 8, (c16 >> 3) == (h >> 4) ? u32x2{hc[2], hc[3]} : u32x2{clk32() - 1000u, tag}, tag, THC);
    TS(7);
    asm volatile("" ::: "memory");
    mv = ld_sc1_64(rs, moff);
    SCHED_FENCE();
    row_issue<2>(rs, off, v);
    SCHED_FENCE();
    wload(7);
    SCHED_FENCE();
    if (UVP < 6) uvread();
    if (UVP == 6) uvwrite();
    if (OWP < 7) owpack();
    u32 lad = L_LAT + w * 288 + c16 * 16 + 4 * r, rsa = L_RS + 16 * (w & 3) + 8 * (w >> 2) + (lane & 4);
    PIN(rsa); PIN(lrd); PIN(mrd);
    PIN(lad); PIN(ooff); PIN(aoff); PIN(cxa); PIN(exw);
    SCHED_FENCE();
    auto bad = [&]() {
      u32 S[2];
#pragma unroll
      for (int t = 0; t < 2; ++t) S[t] = (v[t][0] + v[t][1]) + (v[t][2] + v[t][3]);
      const bool o1 = lane & 1;
      const u32 u0 = (o1 ? S[1] : S[0]) + dppu<0xB1>(o1 ? S[0] : S[1]);
      const u32 s = u0 + dppu<0x4E>(u0);
      return __ballot(tag + mv[0] + s != mv[1]);
    };
    s16x4 cf; float ml;
    auto weights = [&]() {
      float M = __uint_as_float(mv[0]);
      asm volatile("s_nop 1\n\tv_max_f32_dpp %0, %0, %0 quad_perm:[1,0,3,2] row_mask:0xf bank_mask:0xf\n\ts_nop 1" : "+v"(M));
      M = x32_max(x16_max(M));
      const float ex = __builtin_amdgcn_exp2f(__uint_as_float(mv[0]) - M);
      u32 eb = pk2(ex, ex);
      eb = dppb<0x114, 0xa>(eb, eb);
      cf = __builtin_bit_cast(s16x4, u32x2{plo(dppu<0x55>(eb), dppu<0x00>(eb)), 0u});
      float ls = bflo(eb) * __uint_as_float(mv[0]);
      ls += dppf<0xB1>(ls);
      ls = x32_sum(x16_sum(ls));
      ml = (lane & 4) ? ls : M;
      PIN(cf); PIN(ml);
    };
    weights();
    SCHED_FENCE();
    u64 bm = bad();
    if (__builtin_expect(bm != 0ull, 0)) {
#pragma unroll 1
      for (u32 spins = 0; spins < SPIN_CAP; ++spins) {
        __builtin_amdgcn_s_sleep(2);
        asm volatile("" ::: "memory");
        const u32 q = (u32)(bm >> (lane & 60));
        u32x4 t4[2];
#pragma unroll
        for (int t = 0; t < 2; ++t) t4[t] = ld_sc1(rs, ((q >> t) & 1) ? off[t] : OOB);
        const bool rm = (q >> (lane & 3)) & 1;
        const u32x2 t2 = ld_sc1_64(rs, rm ? moff : OOB);
#pragma unroll
        for (int t = 0; t < 2; ++t) v[t] = ((q >> t) & 1) ? t4[t] : v[t];
        mv = rm ? t2 : mv;
        weights();
        bm = bad();
        if (bm == 0ull) break;
      }
    }
    SCHED_FENCE();
    TS(8);
#pragma unroll
    for (int t = 0; t < 2; ++t) v[t][0] -= tag;
    f32x4 d[8];
#pragma unroll
    for (int j = 0; j < 8; ++j) d[j] = __builtin_amdgcn_mfma_f32_16x16x16bf16_1k(cf, __builtin_bit_cast(s16x4, u32x2{(j & 1) ? phi(v[1][j >> 1], v[0][j >> 1]) : plo(v[1][j >> 1], v[0][j >> 1]), 0u}), f32x4{0.f, 0.f, 0.f, 0.f}, 0, 0, 0);
    if ((lane & ~4) == 0) *reinterpret_cast<LDSP float*>((LDSP char*)rsa) = ml;
    SCHED_FENCE();
    wload(8);
    SCHED_FENCE();
#pragma unroll
    for (int j = 0; j < 8; ++j) PIN(d[j]);
    const float x0 = (r & 2) ? ((r & 1) ? d[6][0] : d[4][0]) : ((r & 1) ? d[2][0] : d[0][0]), x1 = (r & 2) ? ((r & 1) ? d[7][0] : d[5][0]) : ((r & 1) ? d[3][0] : d[1][0]);
    *reinterpret_cast<LDSP u32*>((LDSP char*)lad) = pk2(x0, x1);
    if (UVP == 7) uvwrite();
  }
  wg_barrier();
  SCHED_FENCE();
  TS(22);
  {
    if (UVP >= 6) uvread();
    f32x4 c0 = f32x4{0.f, 0.f, 0.f, 0.f}, c1 = c0;
    const f32x4 ml = *reinterpret_cast<const LDSP f32x4*>((const LDSP char*)mrd);
    SCHED_FENCE();
    bf16x8 lf[2][4];
#pragma unroll
    for (int s = 0; s < 4; ++s) { lf[0][s] = *reinterpret_cast<const LDSP bf16x8*>((const LDSP char*)lrd + 64 * s); lf[1][s] = *reinterpret_cast<const LDSP bf16x8*>((const LDSP char*)lrd + 1152 + 64 * s); }
    SCHED_FENCE();
#pragma unroll
    for (int s = 0; s < 4; ++s) { c0 = mfma16(uvf[s], lf[0][s], c0); c1 = mfma16(uvf[s], lf[1][s], c1); }
    const float M = fmaxf(ml[0], ml[2]), a0 = __builtin_amdgcn_exp2f(ml[0] - M), a1 = __builtin_amdgcn_exp2f(ml[2] - M);
    const float rsum = __builtin_amdgcn_rcpf(fmaf(a0, ml[1], a1 * ml[3]));
    const bool i1 = c16 & 4, i2 = c16 & 8;
    float y0 = i1 ? c0[1] : c0[0], y1 = i1 ? c0[3] : c0[2], z0 = i1 ? c1[1] : c1[0], z1 = i1 ? c1[3] : c1[2];
    PIN(y0); PIN(y1); PIN(z0); PIN(z1);
    const float mine = fmaf(a0 * rsum, i2 ? y1 : y0, (a1 * rsum) * (i2 ? z1 : z0));
    TS(9);
    u64 t = fx_add(a.ws, rs, mine, cxa, WS_XC + (u32)((h * 4 + e4) * 512 + tid) * 16, tag);
    if (OWP >= 7) owpack();
#pragma unroll 1
    for (u32 spins = 0; spins < SPIN_CAP; ++spins) {
      if (__all((u32)(t >> 58) == 4u)) break;
      u64 z = 0; PIN(z);
      asm volatile("" ::: "memory");
      if (__builtin_expect(spins < 6, 1)) t = __hip_atomic_load(reinterpret_cast<u64*>(a.ws + cxa), __ATOMIC_RELAXED, __HIP_MEMORY_SCOPE_AGENT);
      else t = __hip_atomic_fetch_add(reinterpret_cast<u64*>(a.ws + cxa), z, __ATOMIC_RELAXED, __HIP_MEMORY_SCOPE_AGENT);
    }
    TS(12);
    const float x = fx_val<4, 8192>(rs, t, WS_XC + (u32)(h * 2048 + tid) * 16, tag);
    const u32 xb = __float_as_uint(x), pa = dppb<0x124, 0xa>(dppb<0x12c, 0x5>(0u, xb), xb);
    const u32 pk = (c16 & 4) ? pk2(__uint_as_float(pa), x) : pk2(x, __uint_as_float(pa));
    const u32 alo = dppb<0x128, 0xc>(pk, pk), ahi = dppb<0x128, 0x3>(pk, pk);
    f32x4 s0 = f32x4{0.f, 0.f, 0.f, 0.f}, s1 = s0;
#pragma unroll
    for (int q = 0; q < 4; ++q) {
      const bool on = (c16 >> 2) == q;
      const s16x4 av = __builtin_bit_cast(s16x4, u32x2{on ? alo : 0u, on ? ahi : 0u});
      s0 = __builtin_amdgcn_mfma_f32_16x16x16bf16_1k(av, owf[2 * q], s0, 0, 0, 0);
      s1 = __builtin_amdgcn_mfma_f32_16x16x16bf16_1k(av, owf[2 * q + 1], s1, 0, 0, 0);
    }
#pragma unroll
    for (int b = 0; b < NB; ++b) *reinterpret_cast<LDSP u32x2*>((LDSP char*)exw + b * 512) = u32x2{__float_as_uint(s0[b]), __float_as_uint(s1[b])};
  }
  wg_barrier();
  {
    const float* pp = reinterpret_cast<const float*>(smem + L_EX) + tid;
    const float y = ((pp[0] + pp[512]) + (pp[1024] + pp[1536])) + ((pp[2048] + pp[2560]) + (pp[3072] + pp[3584]));
    TS(10);
    const u64 t = fx_add(a.ws, rs, y, aoff, WS_XB + h * 32768 + word * 16, tag);
    if ((u32)(t >> 58) == 32u) { const float s = fx_val<32, 32768>(rs, t, WS_XB + word * 16, tag); *reinterpret_cast<u16*>(reinterpret_cast<char*>(a.out) + ooff) = (u16)pk2(s, s); }
  }
  TS(11);
  TS_FLUSH();
}

constexpr int upto(int ch) { const int y = PY + CU * (ch + 1), x = 4 * (ch + AW + 1), z = ch < 0 ? PY : (x > y ? x : y); return z < 4 * NCH ? z : 4 * NCH; }
DI void roleA(const KArgs& a, char* smem, const int g, const int idx, const int w, const int lane, const u32x4 (&R)[4], const u32 kvo, const u32 cpf, const u32 t0, const u64 traw, const u64 ts0_) {
  constexpr int ROLE_ = 1;
  TS_DECL; TSRAW;
  const int r = lane >> 4, c16 = lane & 15, tid = w * 64 + lane;
  const int h = 4 * (idx & 7) + ((idx >> 3) & 3), b = idx >> 5;
  const __amdgpu_buffer_rsrc_t rs = __builtin_amdgcn_make_buffer_rsrc((void*)a.ws, 0, (int)WS_END, RSRC_W3);
  u32 tag = *reinterpret_cast<const u32*>(a.ws + WS_EPOCH);
  u32x4 kv[16][4];
  u32x4 ov[16];
  const u16* wop = a.woa + (size_t)(h * DH + 64 * (c16 >> 3) + 8 * r) * DM + 64 * w + 8 * (c16 & 7);
  auto woload = [&](int n) INL { ov[n] = ldsh(wop + (size_t)(qoffs(n >> 2, b) + (n & 3)) * DM); };
  auto kvissue = [&](auto lo, auto hi) INL {
    unroll_c<hi - lo>([&](auto n0) INL {
      constexpr int n = lo + n0;
      constexpr int ch = n >> 2, isv = ch < C0 ? ch & 1 : ch >= C0 + ND, st = ch < C0 ? ch >> 1 : 8 - ND + (ch - C0) % ND;
      if constexpr (n < PN2) pace<(n < PN1 ? PT0 + n * PD1 : PT0 + PN1 * PD1 + (n - PN1) * PD2)>(t0);
      kv[ch][n & 3] = ldnt(reinterpret_cast<const u16*>(reinterpret_cast<const char*>(isv ? a.vc : a.kc) + (size_t)(((st + (h >> 2)) & 7) * 4096) + 4 * (n & 3) * (DH * 2) + kvo));
      unroll_c<8>([&](auto k) INL { if constexpr (WK0 + k * WKD / 2 == n) { SCHED_FENCE(); woload(k); SCHED_FENCE(); } });
    });
  };
  {
    const int m0 = 128 * b + 16 * w + 4 * r;
    const u32x2 xf = *reinterpret_cast<const u32x2*>(a.xa + (c16 & 3) * DM + m0);
    SCHED_FENCE();
    __builtin_amdgcn_s_barrier();
    SCHED_FENCE();
    kvissue(IC(0), IC(A0));
    SCHED_FENCE();
    asm volatile("" :: "v"(cpf));
    PIN(tag); tag = next_tag(tag);
    f32x4 ac[8];
#pragma unroll
    for (int j = 0; j < 8; ++j) ac[j] = __builtin_amdgcn_mfma_f32_16x16x16bf16_1k(__builtin_bit_cast(s16x4, xf), kfrag(R, j), f32x4{0.f, 0.f, 0.f, 0.f}, 0, 0, 0);
    const f32x4 s0 = sel4(ac[0], ac[2], ac[4], ac[6], r), s1 = sel4(ac[1], ac[3], ac[5], ac[7], r);
    float* pp = reinterpret_cast<float*>(smem + L_AX) + w * 512 + ppos(r, c16);
#pragma unroll
    for (int k = 0; k < NB; ++k) *reinterpret_cast<u32x2*>(pp + k * DH) = u32x2{__float_as_uint(s0[k]), __float_as_uint(s1[k])};
  }
  wg_barrier();
  if (w < 4) {
    const float* pp = reinterpret_cast<const float*>(smem + L_AX) + 2 * tid;
    f32x2_ t[8];
#pragma unroll
    for (int k = 0; k < 8; ++k) t[k] = *reinterpret_cast<const f32x2_*>(pp + 512 * k);
    const f32x2_ s = ((t[0] + t[1]) + (t[2] + t[3])) + ((t[4] + t[5]) + (t[6] + t[7]));
    st_sc1(rs, WS_QX + (u32)(((h * 4 + w) * 4 + b) * DH + 2 * lane) * 8, u32x4{__float_as_uint(s[0]), tag, __float_as_uint(s[1]), tag});
  }
  SCHED_FENCE();
  TS(21);
  __builtin_amdgcn_s_barrier();
  SCHED_FENCE();
  if (NOA) { TS_FLUSH(); return; }
  kvissue(IC(A0), IC(PX));
  SCHED_FENCE();
  u32x4 qpk;
  {
    const u32 off = WS_QX + (u32)(((h * 4 + b) * 4 + ((lane >> 3) & 3)) * DH + 16 * w + 2 * (lane & 7)) * 8;
    u32x4 v = ld_sc1(rs, off);
    SCHED_FENCE();
    kvissue(IC(PX), IC(PY));
    SCHED_FENCE();
    q_finish(rs, off, v, tag, smem + L_AQ + pcol(16 * w + 2 * (lane & 7)) * 2, lane);
    wg_barrier();
    qpk = *reinterpret_cast<const u32x4*>(smem + L_AQ + 16 * c16);
  }
  TS(1);
  SCHED_FENCE();
  float m = -INFINITY, l = 0.f, sd[ND];
  f32x4 acc[8];
  s16x4 pa[ND];
#pragma unroll
  for (int k = 0; k < 8; ++k) acc[k] = f32x4{0.f, 0.f, 0.f, 0.f};
  u32x4 wf[2][8];
  auto wpack = [&](int q) {
#pragma unroll
    for (int j = 0; j < 8; ++j) {
      const u32x2 t = __builtin_bit_cast(u32x2, kfrag(ov + 4 * q, j));
      wf[q >> 1][j][2 * (q & 1)] = t[0]; wf[q >> 1][j][2 * (q & 1) + 1] = t[1];
      PIN(wf[q >> 1][j]);
    }
  };
  const bool hi8 = c16 & 8, hi4 = c16 & 4;
  u32 aaf[4], aml = L_AST + 2304 + 8 * (lane & 7), asw = L_AST + w * 272 + 16 * c16;
#pragma unroll
  for (int q = 0; q < 4; ++q) { aaf[q] = L_AST + (c16 & 7) * 272 + (64 * (c16 >> 3) + 8 * r + qoffs(q, b)) * 2; PIN(aaf[q]); }
  u32 aoff = WS_ACC + b * 16384 + (tid >> 3) * 256 + (tid & 7) * 8, ooff = (b * 1024 + 64 * w + 8 * (c16 & 7) + 2 * r + (c16 >> 3)) * 2;
  PIN(aml); PIN(asw); PIN(aoff); PIN(ooff);
  const u32 foff = WS_FLG + (u32)(idx ^ (lane & 8)) * 128;
  u32x2 fl = u32x2{tag, tag};
  auto wit = [](int n) { return n < 8 ? -1 : n < 12 ? NCH - 1 : NCH; };
  auto wdn = [&](int n) { return (WK0 + n * WKD / 2) / 4 + 2; };
  auto steps = [&](auto lo, auto hi) INL {
  unroll_c<hi - lo>([&](auto ch0) INL {
    constexpr int ch = lo + ch0;
    SCHED_FENCE();
    kvissue(IC(upto(ch - 1)), IC(upto(ch)));
#pragma unroll
    for (int n = 0; n < 16; ++n)
      if (wit(n) == ch) woload(n);
    if (ch == FLI && w == FLW) fl = __builtin_amdgcn_raw_buffer_load_b64(rs, foff, 0, 16);
    SCHED_FENCE();
    if (ASYNC > 0 && ch % ASYNC == ASYNC - 1 && ch < C0 + ND - 1) { __builtin_amdgcn_s_barrier(); SCHED_FENCE(); }
    if constexpr (ch < C0 && !(ch & 1)) {
      const float s = score4(kv[ch], qpk, hi8, hi4);
      if (__builtin_expect(__any(s > m), ch < 2)) {
        float mn = fmaxf(fmaxf(s, dppf<0x128>(s)), m);
        mn = x32_max(x16_max(fmaxf(mn, dppf<0x124>(mn))));
        const float alpha = __builtin_amdgcn_exp2f(m - mn);
        m = mn;
        l *= alpha;
#pragma unroll
        for (int k = 0; k < 8; ++k) acc[k][0] *= alpha;
      }
      const float p = __builtin_amdgcn_exp2f(s - m);
      l += p;
      pa[0] = ppack(p);
      PIN(pa[0]); PIN(l);
    } else if constexpr (ch >= C0 && ch < C0 + ND - 1) {
      sd[ch - C0] = score4(kv[ch], qpk, hi8, hi4);
      PIN(sd[ch - C0]);
    } else if constexpr (ch == C0 + ND - 1) {
      sd[ND - 1] = score4(kv[ch], qpk, hi8, hi4);
      float mn = sd[0];
#pragma unroll
      for (int k = 1; k < ND; ++k) mn = fmaxf(mn, sd[k]);
      mn = fmaxf(fmaxf(mn, dppf<0x128>(mn)), m);
      mn = x32_max(x16_max(fmaxf(mn, dppf<0x124>(mn))));
      const float beta = __builtin_amdgcn_exp2f(m - mn);
      l *= beta;
#pragma unroll
      for (int k = 0; k < ND; ++k) { sd[k] = __builtin_amdgcn_exp2f(sd[k] - mn); l += sd[k]; }
      l += dppf<0x128>(l); l += dppf<0x124>(l);
      l = x32_sum(x16_sum(l));
      if (lane == 0) *reinterpret_cast<f32x2_*>(smem + L_AST + 2304 + 8 * w) = f32x2_{mn, l};
      wg_barrier();
      const f32x2_ ml = *reinterpret_cast<const LDSP f32x2_*>((const LDSP char*)aml);
      float M = fmaxf(ml[0], dppf<0xB1>(ml[0]));
      M = fmaxf(M, dppf<0x4E>(M)); M = fmaxf(M, dppf<0x141>(M));
      float L = __builtin_amdgcn_exp2f(ml[0] - M) * ml[1];
      L += dppf<0xB1>(L); L += dppf<0x4E>(L); L += dppf<0x141>(L);
      const float al = __builtin_amdgcn_exp2f(mn - M) * __builtin_amdgcn_rcpf(L), ab = al * beta;
#pragma unroll
      for (int k = 0; k < 8; ++k) { acc[k][0] *= ab; PIN(acc[k]); }
#pragma unroll
      for (int k = 0; k < ND; ++k) { pa[k] = ppack(sd[k] * al); PIN(pa[k]); }
    } else {
#pragma unroll
      for (int j = 0; j < 8; ++j) acc[j] = __builtin_amdgcn_mfma_f32_16x16x16bf16_1k(pa[ch < C0 ? 0 : ch - C0 - ND], kfrag(kv[ch], j), acc[j], 0, 0, 0);
#pragma unroll
      for (int j = 0; j < 8; ++j) PIN(acc[j]);
    }
#pragma unroll
    for (int q = 0; q < 2; ++q)
      if (wdn(4 * q + 3) - 1 == ch) wpack(q);
  });
  };
  steps(IC(0), IC(NCH - 1));
  if (__builtin_expect(!__all((fl[0] == tag) & (fl[1] == tag)), 0)) {
#pragma unroll 1
    for (u32 spins = 0; spins < SPIN_CAP; ++spins) {
      asm volatile("" ::: "memory");
      fl = __builtin_amdgcn_raw_buffer_load_b64(rs, foff, 0, 16);
      if (__all((fl[0] == tag) & (fl[1] == tag))) break;
      __builtin_amdgcn_s_sleep(2);
    }
  }
  steps(IC(NCH - 1), IC(NCH));
  TS(2);
  if (lane < 16) *reinterpret_cast<LDSP u32x4*>((LDSP char*)asw) = u32x4{pk2(acc[0][0], acc[1][0]), pk2(acc[2][0], acc[3][0]), pk2(acc[4][0], acc[5][0]), pk2(acc[6][0], acc[7][0])};
  SCHED_FENCE();
#pragma unroll
  for (int n = 0; n < 16; ++n)
    if (wit(n) == NCH) woload(n);
  SCHED_FENCE();
  wg_barrier();
  TS(5);
  {
    u32x2 af[4];
#pragma unroll
    for (int q = 0; q < 4; ++q) af[q] = *reinterpret_cast<const LDSP u32x2*>((const LDSP char*)aaf[q]);
    SCHED_FENCE();
    f32x4 D[8];
    auto JO = [](int j) { return (j >> 1) + 4 * (j & 1); };
#pragma unroll
    for (int j = 0; j < 8; ++j) {
      D[j] = mfma16(asbf(u32x4{af[0][0], af[0][1], af[1][0], af[1][1]}), asbf(wf[0][j]), f32x4{0.f, 0.f, 0.f, 0.f});
      SCHED_FENCE();
      wf[1][JO(j)] = __builtin_bit_cast(u32x2, kfrag(ov + 8, JO(j))).xyxy;
      PIN(wf[1][JO(j)][0]); PIN(wf[1][JO(j)][1]);
      SCHED_FENCE();
    }
#pragma unroll
    for (int j = 0; j < 8; ++j) {
      const u32x2 t = __builtin_bit_cast(u32x2, kfrag(ov + 12, JO(j)));
      wf[1][JO(j)][2] = t[0]; wf[1][JO(j)][3] = t[1];
      D[JO(j)] = mfma16(asbf(u32x4{af[2][0], af[2][1], af[3][0], af[3][1]}), asbf(wf[1][JO(j)]), D[JO(j)]);
      SCHED_FENCE();
    }
    float y[8], u[4], v[2];
#pragma unroll
    for (int j = 0; j < 8; ++j) { const f32x2_ t = __builtin_shufflevector(D[JO(j)], D[JO(j)], 0, 1) + __builtin_shufflevector(D[JO(j)], D[JO(j)], 2, 3); y[JO(j)] = t[0] + t[1]; }
#pragma unroll
    for (int k = 0; k < 4; ++k) { auto x = __builtin_amdgcn_permlane32_swap(__float_as_uint(y[k]), __float_as_uint(y[k + 4]), false, false); u[k] = __uint_as_float(hi8 ? x[1] : x[0]); }
#pragma unroll
    for (int k = 0; k < 2; ++k) { auto x = __builtin_amdgcn_permlane16_swap(__float_as_uint(u[k]), __float_as_uint(u[k + 2]), false, false); v[k] = __uint_as_float(x[0]) + __uint_as_float(x[1]); }
    const float yy = (hi8 ? v[1] : v[0]) + dppf<0x128>(hi8 ? v[0] : v[1]);
    TS(3);
    u64 t = fx_add(a.ws, rs, yy, aoff, WS_XA + h * 32768 + (b * 512 + tid) * 16, tag);
    if ((u32)(t >> 58) == 32u) { const float s = fx_val<32, 32768>(rs, t, WS_XA + (b * 512 + tid) * 16, tag); *reinterpret_cast<u16*>(reinterpret_cast<char*>(a.out) + ooff) = (u16)pk2(s, s); }
  }
  TS(11);
  TS_FLUSH();
  asm volatile("s_endpgm" ::: "memory");
}

__global__ void __launch_bounds__(512, 2) fused(KArgs a) {
  const u64 ts0_ = __builtin_amdgcn_s_memrealtime(); SCHED_FENCE();
  extern __shared__ __attribute__((aligned(16))) char smem[];
  const int tid = threadIdx.x, lane = tid & 63, w = __builtin_amdgcn_readfirstlane(tid >> 6), g = blockIdx.x;
  const int idx = (g & 7) | ((g >> 4) << 3);
  const u64 traw = __builtin_amdgcn_s_memrealtime();
  const u32 t0 = (u32)traw;
  u32 cpf = 0;
  if (w >= 8 - CPF && (idx >> 3) < CPN) {
    u64 pc; asm volatile("s_getpc_b64 %0" : "=s"(pc));
    cpf = *reinterpret_cast<const __attribute__((address_space(1))) u32*>((pc & ~127ull) + min((7 - w) * 64 + lane, CPL) * 128);
  }
  SCHED_FENCE();
  u32 o[4], o2;
  {
    const u32 h = 4 * (idx & 7) + ((idx >> 3) & 3), b = idx >> 5, r = lane >> 4;
#pragma unroll
    for (int n = 0; n < 4; ++n) { o[n] = (128 * b + 16 * w + 4 * r + n) * (NH * DH * 2) + h * (DH * 2) + (lane & 15) * 16; PIN(o[n]); }
    u32 ob = (((idx & 3) * SK + ((idx >> 4) + (idx & 8) + 4 * (idx & 4)) * 128 + w) * DC + 8 * lane) * 2, oa = (((b * NH + h) * SK + (SK - WIN) + 128 * w + r) * DH + 8 * (lane & 15)) * 2;
    PIN(oa); PIN(ob);
    o2 = ((g >> 3) & 1) ? ob : oa;
    PIN(o2);
  }
  SCHED_FENCE();
  asm volatile("" :: "s"(a.xa), "s"(a.kc), "s"(a.vc), "s"(a.wqa), "s"(a.woa), "s"(a.xb), "s"(a.ckv), "s"(a.wqb), "s"(a.wuk), "s"(a.wuv), "s"(a.wob), "s"(a.ws), "s"(a.out), "s"(traw));
  u32x4 R[4];
  {
    const char* p = reinterpret_cast<const char*>(((g >> 3) & 1) ? a.wuk : a.wqa);
#pragma unroll
    for (int n = 0; n < 4; ++n) R[n] = ldnt(reinterpret_cast<const u16*>(p + o[n]));
    SCHED_FENCE();
  }
  if (__builtin_expect(!((g >> 3) & 1), 1)) roleA(a, smem, g, idx, w, lane, R, o2, cpf, t0, traw, ts0_);
  else roleB(a, smem, g, idx, w, lane, R, o2, cpf, t0, traw, ts0_);
}

extern "C" void kernel_launch(void* const* d_in, const int* in_sizes, int n_in,
                              void* d_out, int out_size, void* d_ws, size_t ws_size,
                              hipStream_t stream) {
  KArgs a;
  a.xa  = (const u16*)d_in[0];
  a.kc  = (const u16*)d_in[1];
  a.vc  = (const u16*)d_in[2];
  a.wqa = (const u16*)d_in[3];
  a.woa = (const u16*)d_in[4];
  a.xb  = (const u16*)d_in[5];
  a.ckv = (const u16*)d_in[6];
  a.wqb = (const u16*)d_in[7];
  a.wuk = (const u16*)d_in[8];
  a.wuv = (const u16*)d_in[9];
  a.wob = (const u16*)d_in[10];
  {
    const size_t need = (size_t)WS_END + (1u << 20);
    const size_t off = ws_size > need ? (((ws_size - need) / 8 * WOFF) & ~(size_t)0x1fffff) : 0;
    a.ws = (char*)d_ws + off;
  }
  a.out = (u16*)d_out;
  (void)hipFuncSetAttribute((const void*)fused, hipFuncAttributeMaxDynamicSharedMemorySize, L_END);
  fused<<<256, 512, L_END, stream>>>(a);
}
```

```cpp
#define AW 3
#pragma clang diagnostic ignored "-Wint-to-pointer-cast"
#include <hip/hip_runtime.h>
#include <hip/hip_bf16.h>
#include <stdint.h>
#include <utility>

typedef unsigned int u32;
typedef unsigned short u16;
typedef unsigned long long u64;
using u32x4  = __attribute__((ext_vector_type(4))) unsigned;
using u32x2  = __attribute__((ext_vector_type(2))) unsigned;
using f32x4  = __attribute__((ext_vector_type(4))) float;
using bf16x8 = __attribute__((ext_vector_type(8))) short;
using s16x4  = __attribute__((ext_vector_type(4))) short;
#define DI __device__ __forceinline__
#define LDSP __attribute__((address_space(3)))
#define SCHED_FENCE() __builtin_amdgcn_sched_barrier(0)
#define PIN(x) asm volatile("" : "+v"(x))

DI float bflo(u32 v) { return __uint_as_float(v << 16); }
DI float bfhi(u32 v) { return __uint_as_float(v & 0xffff0000u); }
using f32x2_ = __attribute__((ext_vector_type(2))) float;
using bf16x2_ = __attribute__((ext_vector_type(2))) __bf16;
DI u32 pk2(float lo, float hi) { return __builtin_bit_cast(u32, __builtin_convertvector(f32x2_{lo, hi}, bf16x2_)); }
template <int CTRL> DI float dppf(float v) { return __builtin_bit_cast(float, __builtin_amdgcn_update_dpp(0, __builtin_bit_cast(int, v), CTRL, 0xf, 0xf, true)); }
template <int CTRL> DI u32 dppu(u32 v) { return (u32)__builtin_amdgcn_update_dpp(0, (int)v, CTRL, 0xf, 0xf, true); }
DI float row16_sum(float v) { v += dppf<0x128>(v); v += dppf<0x124>(v); v += dppf<0x122>(v); v += dppf<0x121>(v); return v; }
DI float row16_max(float v) { v = fmaxf(v, dppf<0x128>(v)); v = fmaxf(v, dppf<0x124>(v)); v = fmaxf(v, dppf<0x122>(v)); v = fmaxf(v, dppf<0x121>(v)); return v; }
DI float row16_maxq(float v) {
  asm volatile("s_nop 1\n\tv_max_f32_dpp %0, %0, %0 row_ror:8 row_mask:0xf bank_mask:0xf\n\ts_nop 1\n\tv_max_f32_dpp %0, %0, %0 row_ror:4 row_mask:0xf bank_mask:0xf\n\t"
               "s_nop 1\n\tv_max_f32_dpp %0, %0, %0 row_ror:2 row_mask:0xf bank_mask:0xf\n\ts_nop 1\n\tv_max_f32_dpp %0, %0, %0 row_ror:1 row_mask:0xf bank_mask:0xf\n\ts_nop 1" : "+v"(v));
  return v;
}
DI float row16_sumq(float v) {
  asm volatile("s_nop 1\n\tv_add_f32_dpp %0, %0, %0 row_ror:8 row_mask:0xf bank_mask:0xf\n\ts_nop 1\n\tv_add_f32_dpp %0, %0, %0 row_ror:4 row_mask:0xf bank_mask:0xf\n\t"
               "s_nop 1\n\tv_add_f32_dpp %0, %0, %0 row_ror:2 row_mask:0xf bank_mask:0xf\n\ts_nop 1\n\tv_add_f32_dpp %0, %0, %0 row_ror:1 row_mask:0xf bank_mask:0xf\n\ts_nop 1" : "+v"(v));
  return v;
}
DI float max3q(float a, float b, float c) { float d; asm("v_max3_f32 %0, %1, %2, %3" : "=v"(d) : "v"(a), "v"(b), "v"(c)); return d; }
DI float max8q(f32x4 a, f32x4 b) { return max3q(max3q(a[0], a[1], a[2]), max3q(a[3], b[0], b[1]), max3q(b[2], b[3], b[3])); }
DI float x64_maxq(float v) {
  auto s = __builtin_amdgcn_permlane16_swap(__float_as_uint(v), __float_as_uint(v), false, false);
  v = max3q(__uint_as_float(s[0]), __uint_as_float(s[1]), __uint_as_float(s[1]));
  auto t = __builtin_amdgcn_permlane32_swap(__float_as_uint(v), __float_as_uint(v), false, false);
  return max3q(__uint_as_float(t[0]), __uint_as_float(t[1]), __uint_as_float(t[1]));
}
DI float x16_sum(float v) { auto s = __builtin_amdgcn_permlane16_swap(__float_as_uint(v), __float_as_uint(v), false, false); return __uint_as_float(s[0]) + __uint_as_float(s[1]); }
DI float x32_sum(float v) { auto s = __builtin_amdgcn_permlane32_swap(__float_as_uint(v), __float_as_uint(v), false, false); return __uint_as_float(s[0]) + __uint_as_float(s[1]); }
DI float x16_max(float v) { auto s = __builtin_amdgcn_permlane16_swap(__float_as_uint(v), __float_as_uint(v), false, false); return fmaxf(__uint_as_float(s[0]), __uint_as_float(s[1])); }
DI float x32_max(float v) { auto s = __builtin_amdgcn_permlane32_swap(__float_as_uint(v), __float_as_uint(v), false, false); return fmaxf(__uint_as_float(s[0]), __uint_as_float(s[1])); }
using bf16x2 = __attribute__((ext_vector_type(2))) __bf16;
DI float dot2(u32 a, u32 b, float c) { return __builtin_amdgcn_fdot2_f32_bf16(__builtin_bit_cast(bf16x2, a), __builtin_bit_cast(bf16x2, b), c, false); }
DI u32 plo(u32 r1, u32 r0) { return __builtin_amdgcn_perm(r1, r0, 0x05040100u); }
DI u32 phi(u32 r1, u32 r0) { return __builtin_amdgcn_perm(r1, r0, 0x07060302u); }
DI bf16x8 bfrag(const u32x4* R, int j) {
  const int q = j >> 1;
  u32x4 o;
  if (j & 1) { o[0] = phi(R[1][q], R[0][q]); o[1] = phi(R[3][q], R[2][q]); o[2] = phi(R[5][q], R[4][q]); o[3] = phi(R[7][q], R[6][q]); }
  else       { o[0] = plo(R[1][q], R[0][q]); o[1] = plo(R[3][q], R[2][q]); o[2] = plo(R[5][q], R[4][q]); o[3] = plo(R[7][q], R[6][q]); }
  return __builtin_bit_cast(bf16x8, o);
}
DI s16x4 kfrag(const u32x4* R, int j) {
  const int q = j >> 1;
  u32x2 o;
  if (j & 1) { o[0] = phi(R[1][q], R[0][q]); o[1] = phi(R[3][q], R[2][q]); }
  else       { o[0] = plo(R[1][q], R[0][q]); o[1] = plo(R[3][q], R[2][q]); }
  return __builtin_bit_cast(s16x4, o);
}
DI f32x4 sel4(f32x4 a, f32x4 b, f32x4 c, f32x4 d, int r) { f32x4 lo = (r & 1) ? b : a, hi = (r & 1) ? d : c; return (r & 2) ? hi : lo; }
DI f32x4 mfma16(bf16x8 a, bf16x8 b, f32x4 c) { return __builtin_amdgcn_mfma_f32_16x16x32_bf16(a, b, c, 0, 0, 0); }
DI bf16x8 asbf(u32x4 v) { return __builtin_bit_cast(bf16x8, v); }
DI float dot8(const u32x4 kv, const u32x4 q) {
  float s = dot2(kv[0], q[0], 0.f);
  s = dot2(kv[1], q[1], s); s = dot2(kv[2], q[2], s); s = dot2(kv[3], q[3], s);
  return s;
}
constexpr int NB = 4, NH = 32, DH = 128, SK = 4096, WIN = 1024, DM = 512, DC = 512;
constexpr float LOG2E = 1.4426950408889634f;
constexpr float SCL2 = 0.088388347648318440f * LOG2E;

#define WOFF 8
#define ASYNC 2
#define A0 4
#define PX 14
#define PY 22
#define CU 2
#define FLI 10
#define FLW 0
#define UVP 3
#define OWP 8
#define CPF 3
#define CPN 1
#define CPL 191
#define ND 3
#define THC 325
#define PT0 900
#define PD1 270
#define PN1 12
#define PD2 248
#define PN2 38
#define PD3 200
#define PN3 PN2
#define SKA 0
#define SKB SKA
#define WIB 0
#define WIO 0
#define WS1 15
#define WS2 16
#define CO 0
#define CON 1
#define COB 1
#define COM 150
#define KM 0
#define TM 0
#define QM 0
#define TAX 0
#define SAX 0
#define SHP 0
#define BVE 12
#define WP0 0
#define QP0 0
#define WPB -1
#define WPS 0
#define BT0 900
#define BD1 440
#define BDQ 60
#define BD2 390
#define XR 4
#define UKR 5
#define X0 8
#define TI 3
#define T2N 1
#define TH 13
#define T4N 1
#define WLG 3
#define CK1 8
#define QA 3
#define TFS 4
#define WK0 17
#define WKD 7
#define NOA 0
#define SIBS 0, 0, 0, 0, 4, 4, 4, 4, 8, 8, 8, 8, 8, 8, 8, 8
constexpr int sibs_[17] = {SIBS, 16};
constexpr int sb(int i) { return i < 0 ? 0 : sibs_[i]; }
constexpr int C0 = 16 - 2 * ND;
constexpr int NCH = 16;
constexpr u32 WS_EPOCH = 0;
constexpr u32 WS_QP    = 4096;
constexpr u32 PBLK     = 4608;
constexpr u32 WS_PART  = WS_QP + NB * 8 * 384 * 16;
constexpr u32 WS_QX    = WS_PART + NB * 16 * 8 * PBLK;
constexpr u32 WS_ACC   = WS_QX + NH * NB * 4 * DH * 8;
constexpr u32 WS_ACX   = WS_ACC + 2 * 65536;
constexpr u32 WS_FLG   = WS_ACX + 32 * 4096;
constexpr u32 WS_XA    = WS_FLG + 128 * 128;
constexpr u32 WS_XB    = WS_XA + 32 * 2048 * 16;
constexpr u32 WS_XC    = WS_XB + 32 * 2048 * 16;
constexpr u32 WS_END   = WS_XC + 32 * 2048 * 16;
constexpr u32 WS_TS    = WS_END;
constexpr u32 RSRC_W3  = 0x00020000;
constexpr u32 SPIN_CAP = 1u << 17;

constexpr int L_T    = 0;
constexpr int L_Q    = 131072;
constexpr int L_P    = L_Q + 16384;
constexpr int L_MX   = L_P + 8192;
constexpr int L_UKT  = L_Q;
constexpr int L_S1P  = L_Q + 4096;
constexpr int L_QB   = L_Q;
constexpr int L_QO   = L_Q + 2048;
constexpr int L_LAT  = L_Q;
constexpr int L_RS   = L_LAT + 2304;
constexpr int L_EX   = L_Q + 16384;
constexpr int L_ENDB = 163840;
constexpr int L_AP   = 0;
constexpr int L_AQ   = L_AP + 4096;
constexpr int L_AST  = L_AQ + 2048;
constexpr int L_AR2  = L_AST + 4352;
constexpr int L_AX   = L_AR2 + 16384;
constexpr int L_END  = L_ENDB;

DI u32 fsw(u32 key) { return (key & 2) | ((key >> 1) & 4); }
DI int ppos(int r, int c16) { return 32 * r + 2 * c16; }
DI int pcol(int p) { return 8 * ((p >> 1) & 15) + 2 * (p >> 5) + (p & 1); }
struct KArgs {
  const u16* xa; const u16* kc; const u16* vc; const u16* wqa; const u16* woa;
  const u16* xb; const u16* ckv; const u16* wqb; const u16* wuk; const u16* wuv; const u16* wob;
  char* ws; u16* out;
};

DI u32x4 ldnt(const u16* p) { return __builtin_nontemporal_load(reinterpret_cast<const u32x4*>(p)); }
DI u32x4 ldsh(const u16* p) { return SHP ? ldnt(p) : *reinterpret_cast<const u32x4*>(p); }
DI u32x4 ld_sc1(__amdgpu_buffer_rsrc_t rs, u32 off) { return __builtin_amdgcn_raw_buffer_load_b128(rs, off, 0, 16); }
DI u32x2 ld_sc1_64(__amdgpu_buffer_rsrc_t rs, u32 off) { return __builtin_amdgcn_raw_buffer_load_b64(rs, off, 0, 16); }
DI void st_sc1(__amdgpu_buffer_rsrc_t rs, u32 off, u32x4 v) { __builtin_amdgcn_raw_buffer_store_b128(v, rs, off, 0, 16); }
DI void st_sc1_32(__amdgpu_buffer_rsrc_t rs, u32 off, u32 v) { __builtin_amdgcn_raw_buffer_store_b32(v, rs, off, 0, 16); }
DI void wg_barrier() { asm volatile("s_waitcnt lgkmcnt(0)\n\ts_barrier" ::: "memory"); }

#define TS(i)
#define TSRAW
#define TSARG
#define TSPASS
#define TS_DECL
#define TS_FLUSH()
DI u64 fx_add(char* ws, __amdgpu_buffer_rsrc_t rs, float y, u32 aoff, u32 xown, u32 tag) {
  const float t = __builtin_fabsf(y);
  u64 x = __builtin_bit_cast(u64, __builtin_fma((double)y, 0x1p40, 0x1.8p52)) + (0x0400000000000000ull - 0x4338000000000000ull);
  PIN(x);
  const u64 big = __ballot(!(t < 64.f)), some = __ballot(t >= 0x1p-24f);
  u64 odd;
  asm("s_cmp_eq_u64 %2, 0\n\ts_cselect_b64 %0, -1, %1" : "=s"(odd) : "s"(big), "s"(some) : "scc");
  if (__builtin_expect(odd != 0ull, 0)) {
    if (__ballot(t != 0.f) != 0ull) {
      st_sc1(rs, xown, u32x4{__float_as_uint(y), tag, ~__float_as_uint(y), tag});
      x = 0x0410000000000000ull;
      asm volatile("s_waitcnt vmcnt(0)" ::: "memory");
    }
  }
  return __hip_atomic_fetch_add(reinterpret_cast<u64*>(ws + aoff), x, __ATOMIC_RELAXED, __HIP_MEMORY_SCOPE_AGENT) + x;
}
template <int NARR, int XSTR>
DI float fx_val(__amdgpu_buffer_rsrc_t rs, u64 t, u32 x0, u32 tag) {
  float s = (float)__builtin_fma(__builtin_bit_cast(double, (t & 0xfffffffffffffull) | 0x4330000000000000ull), 0x1p-40, -0x1.8p12);
  const u32 np = (u32)(t >> 52) & 63u;
  if (__builtin_expect(np != 0u, 0)) {
    float e = 0.f;
#pragma unroll 1
    for (u32 spins = 0; spins < 64; ++spins) {
      u32 c = 0; e = 0.f;
#pragma unroll 4
      for (u32 p = 0; p < NARR; ++p) {
        const u32x4 v = ld_sc1(rs, x0 + p * XSTR);
        const bool ok = (v[1] == tag) & (v[3] == tag) & (v[0] == ~v[2]);
        e += ok ? __uint_as_float(v[0]) : 0.f; c += ok;
      }
      if (c == np) break;
    }
    s += e;
  }
  return s;
}
template <int CTRL, int BANK> DI u32 dppb(u32 old, u32 v) { return (u32)__builtin_amdgcn_update_dpp((int)old, (int)v, CTRL, 0xf, BANK, false); }
constexpr u32 OOB = 0x80000000u;
template <int N>
DI void row_issue(__amdgpu_buffer_rsrc_t rs, const u32 (&off)[N], u32x4 (&v)[N]) {
#pragma unroll
  for (int p = 0; p < N; ++p) v[p] = ld_sc1(rs, off[p]);
}
template <int N>
DI void row_wait(__amdgpu_buffer_rsrc_t rs, const u32 (&off)[N], u32x4 (&v)[N], u32 tag, bool act, int lane) {
  bool ok = true;
#pragma unroll
  for (int p = 0; p < N; ++p) PIN(v[p]);
#pragma unroll
  for (int p = 0; p < N; ++p) ok &= (v[p][3] == tag);
  if (__builtin_expect(!__all(ok | !act), 0)) {
#pragma unroll 1
    for (u32 spins = 0; spins < SPIN_CAP; ++spins) {
      __builtin_amdgcn_s_sleep(2);
      asm volatile("" ::: "memory");
      u32x4 t[N]; bool re[N];
#pragma unroll
      for (int p = 0; p < N; ++p) {
        const u64 m = __ballot(act & !((v[p][3] == tag)));
        re[p] = ((u32)(m >> (lane & 48)) & 0xffffu) != 0u;
        t[p] = ld_sc1(rs, re[p] ? off[p] : OOB);
      }
      bool k2 = true;
#pragma unroll
      for (int p = 0; p < N; ++p) { v[p] = re[p] ? t[p] : v[p]; PIN(v[p]); k2 &= (v[p][3] == tag); }
      if (__all(k2 | !act)) break;
    }
  }
}
DI u32 early_start(const char* ws, u64 traw, u32 t0) {
  u64 wp = reinterpret_cast<u64>(ws) + 2048;
  asm volatile("" : "+s"(wp));
  using u64x8 = __attribute__((ext_vector_type(8))) u64;
  const u64x8 x = *reinterpret_cast<const __attribute__((address_space(4))) u64x8*>(wp);
  u32 d = 0;
#pragma unroll
  for (int k = 0; k < 8; ++k) { const u64 e = traw - x[k]; d = e < (u64)COM ? max((u32)e, d) : d; }
  return t0 - d;
}
template <int NS> DI void pace(u32 t0) {
  SCHED_FENCE();
  if (NS > 0)
    asm volatile("s_branch .Lpc%=\n.Lpl%=:\n\ts_sleep 1\n.Lpc%=:\n\ts_memrealtime s[98:99]\n\ts_waitcnt lgkmcnt(0)\n\ts_sub_u32 s98, s98, %0\n\ts_cmpk_lt_u32 s98, %1\n\ts_cbranch_scc1 .Lpl%="
                 :: "s"(t0), "n"(NS / 10) : "s98", "s99", "scc", "memory");
  SCHED_FENCE();
}
template <class F, int... I> DI void unroll_i(F f, std::integer_sequence<int, I...>) { (f(std::integral_constant<int, I>{}), ...); }
template <int N, class F> DI void unroll_c(F f) { unroll_i(f, std::make_integer_sequence<int, N>{}); }
#define IC(x) std::integral_constant<int, x>{}
#define INL __attribute__((always_inline))
DI int lane_id() { int l; asm volatile("v_mbcnt_lo_u32_b32 %0, -1, 0\n\tv_mbcnt_hi_u32_b32 %0, -1, %0" : "=v"(l)); return l; }
DI u32 clk32() { return (u32)__builtin_amdgcn_s_memrealtime(); }
DI void hint_wait(__amdgpu_buffer_rsrc_t rs, u32 off, u32x2 hv, u32 tag, int dt) {
  PIN(hv);
  if (__builtin_expect(!__all(hv[1] == tag), 0)) {
#pragma unroll 1
    for (u32 spins = 0; spins < 8; ++spins) {
      asm volatile("" ::: "memory");
      const u32x2 t = ld_sc1_64(rs, hv[1] != tag ? off : OOB);
      hv = hv[1] != tag ? t : hv;
      PIN(hv);
      if (__all(hv[1] == tag)) break;
    }
  }
  const u32 t0 = clk32();
  const float d = row16_maxq(hv[1] == tag ? (float)(int)(hv[0] - t0) : -1000.f);
  const int n = min(__builtin_amdgcn_readfirstlane((int)d) + dt, 250);
#pragma unroll 1
  for (int i = 0; i < 256 && (int)(clk32() - t0) < n; ++i) __builtin_amdgcn_s_sleep(1);
  asm volatile("" ::: "memory");
}
DI void both_halves(__amdgpu_buffer_rsrc_t rs, u32 off, u32x4& hv, u32 tag) {
  PIN(hv);
  u32 m = (u32)__ballot((hv[0] == tag) & (hv[1] == tag) & (hv[3] == tag));
  if (__builtin_expect(!(m & 0xffu) | !(m & 0xff00u), 0)) {
#pragma unroll 1
    for (u32 spins = 0; spins < SPIN_CAP; ++spins) {
      __builtin_amdgcn_s_sleep(2);
      asm volatile("" ::: "memory");
      hv = ld_sc1(rs, off);
      PIN(hv);
      m = (u32)__ballot((hv[0] == tag) & (hv[1] == tag) & (hv[3] == tag));
      if ((m & 0xffu) && (m & 0xff00u)) break;
    }
  }
}
DI u32 next_tag(u32 e) { return e * 1664525u + 1013904223u; }

DI void s1_mfma(const u32x4 (&R)[2][8], const u32x4 (&xf)[2], int r, f32x4& s0, f32x4& s1) {
  f32x4 acc[8];
#pragma unroll
  for (int j = 0; j < 8; ++j) acc[j] = mfma16(asbf(xf[0]), bfrag(R[0], j), f32x4{0.f, 0.f, 0.f, 0.f});
#pragma unroll
  for (int j = 0; j < 8; ++j) PIN(acc[j]);
  SCHED_FENCE();
#pragma unroll
  for (int j = 0; j < 8; ++j) acc[j] = mfma16(asbf(xf[1]), bfrag(R[1], j), acc[j]);
  s0 = sel4(acc[0], acc[2], acc[4], acc[6], r); s1 = sel4(acc[1], acc[3], acc[5], acc[7], r);
}
DI int qoffs(int qd, int rot) { const int aq = (qd + rot) & 3; return 32 * (aq >> 1) + 4 * (aq & 1); }
DI void x_load(const u16* xsrc, int rot, u32x4 (&xf)[2]) {
#pragma unroll
  for (int k = 0; k < 2; ++k) {
    const u32x2 lo = *reinterpret_cast<const u32x2*>(xsrc + qoffs(2 * k, rot)), hi = *reinterpret_cast<const u32x2*>(xsrc + qoffs(2 * k + 1, rot));
    xf[k] = u32x4{lo[0], lo[1], hi[0], hi[1]};
  }
}
constexpr int QS = QP0 ? 0 : 4;
constexpr int npl(int i) { int c = 0; for (int k = 0; k < 4; ++k) c += WPB >= 0 && WPB + k * WPS <= i; return c; }
constexpr int trow_t(int i) { return i < X0 ? BT0 + (i + (i >= XR ? QS : 0) + npl(i)) * BD1 : BT0 + (X0 + QS + npl(X0)) * BD1 + 12 * BDQ + (i - X0) * BD2; }
constexpr int wq_t(int n) { return n < 4 ? (QP0 ? 0 : BT0 + (XR + npl(XR - 1) + n) * BD1) : BT0 + (X0 + QS + npl(X0)) * BD1 + (n - 4) * BDQ; }
template <int LO, int HI> DI void s1_load(const u16* wsrc, int rot, u32x4 (&R)[2][8], u32 t0) {
  unroll_c<4 * (HI - LO)>([&](auto k) {
    constexpr int n = 4 * LO + k, qd = n >> 2, i = n & 3;
    if (i == 0 && qd) { SCHED_FENCE(); __builtin_amdgcn_s_barrier(); }
    pace<wq_t(n)>(t0);
    R[qd >> 1][4 * (qd & 1) + i] = ldsh(wsrc + (size_t)(qoffs(qd, rot) + i) * (NH * DH));
  });
}

DI float score4(const u32x4 (&k)[4], const u32x4& qpk, bool hi8, bool hi4) {
  const float d0 = dot8(k[0], qpk), d1 = dot8(k[1], qpk), d2 = dot8(k[2], qpk), d3 = dot8(k[3], qpk);
  const float a0 = (hi8 ? d2 : d0) + dppf<0x128>(hi8 ? d0 : d2), a1 = (hi8 ? d3 : d1) + dppf<0x128>(hi8 ? d1 : d3);
  float s = (hi4 ? a1 : a0) + dppf<0x141>(hi4 ? a0 : a1);
  s += dppf<0xB1>(s); s += dppf<0x4E>(s);
  return s * SCL2;
}
DI s16x4 ppack(float p) { return __builtin_bit_cast(s16x4, u32x2{pk2(p, dppf<0x12c>(p)), pk2(dppf<0x128>(p), dppf<0x124>(p))}); }
DI void q_finish(__amdgpu_buffer_rsrc_t rs, u32 off, u32x4 v, u32 tag, char* dst, int lane) {
  PIN(v);
  if (__builtin_expect(!__all((v[1] == tag) & (v[3] == tag)), 0)) {
#pragma unroll 1
    for (u32 spins = 0; spins < SPIN_CAP; ++spins) {
      asm volatile("" ::: "memory");
      v = ld_sc1(rs, off);
      PIN(v);
      if (__all((v[1] == tag) & (v[3] == tag))) break;
      __builtin_amdgcn_s_sleep(1);
    }
  }
  float q0 = __uint_as_float(v[0]), q1 = __uint_as_float(v[2]);
  q0 += dppf<0x128>(q0); q1 += dppf<0x128>(q1);
  q0 = x16_sum(q0); q1 = x16_sum(q1);
  if (lane < 8) *reinterpret_cast<u32*>(dst) = pk2(q0, q1);
}

DI void roleB(const KArgs& a, char* smem, const int g, const int idx, const int w, const int lane0, const u32x4 (&uk)[4], const u32 tvo, const u32 cpf, u32 t0, const u64 traw) {
  TS_DECL; TSRAW;
  const int h = 4 * (idx & 7) + ((idx >> 3) & 3), e4 = idx >> 5;
  const int b3 = idx & 3, kp = 2 * (idx >> 4) + ((idx >> 2) & 1), hh = (idx >> 3) & 1;
  const __amdgpu_buffer_rsrc_t rs = __builtin_amdgcn_make_buffer_rsrc((void*)a.ws, 0, (int)WS_END, RSRC_W3);
  const __amdgpu_buffer_rsrc_t rt = __builtin_amdgcn_make_buffer_rsrc((void*)a.ckv, 0, NB * SK * DC * 2, RSRC_W3);
  u32 tag = *reinterpret_cast<const u32*>(a.ws + WS_EPOCH);
  u32x4 tv[32], uv[4], ow[4];
  auto wload = [&](int pos) INL {
    if (pos == UVP) {
      const int l = lane_id();
      const char* p = reinterpret_cast<const char*>(a.wuv + ((size_t)(128 * e4 + 16 * w) * NH + h) * DH);
      const u32 o = (l >> 4) * (8 * NH * DH) + (l & 15) * 16;
#pragma unroll
      for (int n = 0; n < 4; ++n) uv[n] = ldnt(reinterpret_cast<const u16*>(p + n * (NH * DH * 2) + o));
    }
    if (pos == OWP) {
      const int l = lane_id();
      const char* p = reinterpret_cast<const char*>(a.wob + (size_t)(h * DH + 16 * w) * DM + 128 * e4);
      const u32 o = (l >> 4) * (8 * DM) + (l & 15) * 16;
#pragma unroll
      for (int n = 0; n < 4; ++n) ow[n] = ldnt(reinterpret_cast<const u16*>(p + n * (DM * 2) + o));
    }
  };
  const u32 tvl = tvo & ~(8u << 17), tso = (u32)hh << 20, tro = TM ? (((idx >> 4) & 2) | ((idx & 4) << 1)) ^ ((idx >> 6) * 10) : (idx >> 4) + (idx & 3);
  auto cluster = [&](auto i) INL {
    unroll_c<sb(i) - sb(i - 1)>([&](auto k0) INL { constexpr int k = sb(i - 1) + k0; tv[2 * (k & 8) + 8 + (k & 7)] = __builtin_amdgcn_raw_buffer_load_b128(rt, tvl, (tso ^ (1u << 20)) + (TM ? (k ^ tro) * 4096 : ((k + tro) & 15) * 8192), SAX); });
  };
  auto tload = [&](auto lo, auto hi) INL {
    unroll_c<hi - lo>([&](auto i0) INL {
      constexpr int i = lo + i0;
      pace<trow_t(i)>(t0);
      if constexpr (i != X0) cluster(IC(i));
      tv[2 * (i & 8) + (i & 7)] = __builtin_amdgcn_raw_buffer_load_b128(rt, tvl, tso + (TM ? (i ^ tro) * 4096 : ((i + tro) & 15) * 8192), TAX);
      SCHED_FENCE();
      if constexpr (CO && i == COB) { t0 = early_start(a.ws, traw, t0); SCHED_FENCE(); }
    });
  };
  if (w >= 6) st_sc1(rs, WS_ACC + (u32)((idx * 2 + w - 6) * 64 + lane0) * 16, u32x4{0u, 0x80000u, 0u, 0x80000u});
  SCHED_FENCE();
  constexpr int QN = 3;
  u32x4 qv[QN]; u32 qoff, pa64[QN], pa32[QN], ack, atf[2], atw;
  {
    const int lane = lane0, r = lane >> 4, c16 = lane & 15;
#pragma unroll
    for (int k = 0; k < QN; ++k) {
      const int G = 64 * k + lane, hl = 8 * (w & 1) + G / 24, rec = G % 24, u = rec / 3, j = rec - 3 * u, sc = 16 * (w >> 1) + 2 * u;
      pa64[k] = L_Q + hl * 1024 + (((sc + (j >> 1)) ^ hl) << 4) + 8 * (j & 1);
      pa32[k] = L_Q + hl * 1024 + (((sc + 1) ^ hl) << 4) + (j == 2 ? 4 : 8 + 4 * j);
      PIN(pa64[k]); PIN(pa32[k]);
    }
    ack = L_T + (16 * w + c16) * 128 + ((r ^ fsw(c16)) << 4);
    const u32 tq = c16 >> 2, tp = lane & 3;
#pragma unroll
    for (int u = 0; u < 2; ++u) { const u32 rr = 8 * r + tq + 4 * u; atf[u] = L_T + w * 16384 + rr * 128 + (((tp >> 1) ^ fsw(rr)) << 4) + 8 * (tp & 1); PIN(atf[u]); }
    atw = L_T + (lane >> 3) * 16384 + w * 128 + (((lane & 7) ^ fsw(w)) << 4);
    PIN(ack); PIN(atw);
  }
  auto twrite = [&](int half, int lo, int hi) INL {
    SCHED_FENCE();
    u32 aw[2] = {atw, atw};
    PIN(aw[1]);
    aw[1] ^= 64u;
#pragma unroll
    for (int i = 0; i < 17; ++i) {
      if (i < lo || i >= hi) continue;
#pragma unroll
      for (int k = 0; k < 16; ++k)
        if (k >= sb(i - 1) && k < sb(i) && (k >> 3) == half) *reinterpret_cast<LDSP u32x4*>((LDSP char*)aw[k & 1] + (8 + (k & 7)) * 1024) = tv[2 * (k & 8) + 8 + (k & 7)];
      if (i < 16 && (i >> 3) == half) *reinterpret_cast<LDSP u32x4*>((LDSP char*)aw[i & 1] + (i & 7) * 1024) = tv[2 * (i & 8) + (i & 7)];
    }
    SCHED_FENCE();
  };
  SCHED_FENCE();
  {
    const int lane = lane0, tid = w * 64 + lane, r = lane >> 4, c16 = lane & 15;
    u32x4 ua[4];
    const int m0 = 64 * w + 8 * r;
    const u16* wsrc = a.wqb + (size_t)m0 * (NH * DH) + h * DH + 8 * c16;
    u32x4 R[2][8], xf[2];
    u32 wpl[4] = {0, 0, 0, 0};
    const u16* wps = a.woa + (size_t)(h * DH + 64 * (w >> 2) + 8 * (w & 3) + qoffs(0, e4)) * DM + 64 * (lane & 7);
    static_assert(X0 >= 8 && TH >= X0 + TI + T2N && sb(X0) >= 8);
    x_load(a.xb + (c16 & 3) * DM + m0, e4, xf);
    unroll_c<X0 + 1>([&](auto i) INL {
      if constexpr (i == UKR) {
        SCHED_FENCE();
        asm volatile("" :: "v"(cpf));
        tag = next_tag(__builtin_amdgcn_readfirstlane(tag));
        char* us = smem + L_UKT + w * 4096;
#pragma unroll
        for (int k = 0; k < 4; ++k) *reinterpret_cast<u32x4*>(us + (4 * r + k) * 256 + ((c16 ^ (4 * r + k)) << 4)) = uk[k];
        asm volatile("" ::: "memory");
#pragma unroll
        for (int k = 0; k < 4; ++k) ua[k] = *reinterpret_cast<const u32x4*>(us + c16 * 256 + (((4 * k + r) ^ c16) << 4));
#pragma unroll
        for (int k = 0; k < 4; ++k) PIN(ua[k]);
      }
      if constexpr (i == (QP0 ? 0 : XR)) s1_load<0, 1>(wsrc, e4, R, t0);
      unroll_c<4>([&](auto k) INL { if constexpr (WPB >= 0 && i == WPB + k * WPS) { pace<BT0 + (i + (i >= XR ? QS : 0) + k) * BD1>(t0); wpl[k] = *reinterpret_cast<const u32*>(wps + k * DM); SCHED_FENCE(); } });
      if constexpr (i < X0) tload(i, IC(i + 1));
      if constexpr (i < X0 && i >= WLG) twrite(0, i - WLG, i - WLG + 1);
    });
    pace<wq_t(4)>(t0);
    cluster(IC(X0));
    s1_load<1, 4>(wsrc, e4, R, t0);
    SCHED_FENCE();
    tload(IC(X0), IC(X0 + TI));
    twrite(0, X0 - WLG, X0 + 1);
    f32x4 s0, s1;
    asm volatile("" :: "v"(wpl[0]), "v"(wpl[1]), "v"(wpl[2]), "v"(wpl[3]));
    s1_mfma(R, xf, r, s0, s1);
    float* pw = reinterpret_cast<float*>(smem + L_S1P) + w * 512 + ppos(r, c16);
#pragma unroll
    for (int b = 0; b < NB; ++b) *reinterpret_cast<u32x2*>(pw + b * DH) = u32x2{__float_as_uint(s0[b]), __float_as_uint(s1[b])};
    SCHED_FENCE();
    tload(IC(X0 + TI), IC(X0 + TI + T2N));
    wg_barrier();
    SCHED_FENCE();
    tload(IC(X0 + TI + T2N), IC(TH));
    const float* pp = reinterpret_cast<const float*>(smem + L_S1P) + tid;
    const float s = ((pp[0] + pp[512]) + (pp[1024] + pp[1536])) + ((pp[2048] + pp[2560]) + (pp[3072] + pp[3584]));
    *reinterpret_cast<u16*>(smem + L_QB + (tid >> 7) * 288 + pcol(tid & 127) * 2) = (u16)pk2(s, s);
    wg_barrier();
    SCHED_FENCE();
    tload(IC(TH), IC(TH + T4N));
    f32x4 d0 = f32x4{0.f, 0.f, 0.f, 0.f};
#pragma unroll
    for (int i = 0; i < 4; ++i) {
      const bf16x8 qf = *reinterpret_cast<const bf16x8*>(smem + L_QB + (c16 & 3) * 288 + (32 * i + 8 * r) * 2);
      d0 = mfma16(asbf(ua[i]), qf, d0);
    }
    if (c16 < 4) {
      char* o = smem + L_QO + w * 256 + c16 * 48;
      *reinterpret_cast<u32*>(o + (r < 3 ? 16 * r : 8)) = pk2(d0[0], d0[1]);
      *reinterpret_cast<u32*>(o + (r < 3 ? 16 * r + 4 : 24)) = pk2(d0[2], d0[3]);
    }
    wg_barrier();
    if (w < 4 && lane < 24) {
      u32x4 x = *reinterpret_cast<const u32x4*>(smem + L_QO + (lane / 3) * 256 + (3 * w + lane % 3) * 16);
      x[3] = tag; x[2] = lane % 3 == 2 ? x[1] : x[2];
      st_sc1(rs, WS_QP + (u32)(((w * 4 + e4) * 32 + h) * 24 + lane) * 16, x);
    }
  }
  SCHED_FENCE();
  TS(2);
  u32x4 ck[2][16];
  u32 ac[4], at[4][2];
  auto ckaddr = [&]() INL { PIN(ack); ac[0] = ack; ac[1] = ack ^ 64u; ac[2] = ac[0] + 65536u; ac[3] = ac[1] + 65536u; };
  auto tfaddr = [&]() INL {
#pragma unroll
    for (int u = 0; u < 2; ++u) {
      PIN(atf[u]);
#pragma unroll
      for (int ct = 0; ct < 4; ++ct) at[ct][u] = atf[u] ^ (u32)(ct << 5);
    }
  };
  auto ckread = [&](int kb, int s) INL { ck[kb][s] = *reinterpret_cast<const LDSP u32x4*>((const LDSP char*)ac[2 * (s >> 3) + (s & 1)] + ((s >> 1) & 3) * 16384); };
  bf16x8 tf[2][8][2];
  auto tfread = [&](int kb, int k2) INL {
#pragma unroll
    for (int cb = 0; cb < 2; ++cb) {
      s16x4 b0 = __builtin_amdgcn_ds_read_tr16_b64_v4i16((LDSP s16x4*)((LDSP char*)at[2 * (k2 & 1) + cb][0] + 4096 * (k2 >> 1)));
      s16x4 b1 = __builtin_amdgcn_ds_read_tr16_b64_v4i16((LDSP s16x4*)((LDSP char*)at[2 * (k2 & 1) + cb][1] + 4096 * (k2 >> 1)));
      tf[kb][k2][cb] = __builtin_shufflevector(b0, b1, 0, 1, 2, 3, 4, 5, 6, 7);
    }
  };
  constexpr int R0 = TH + T4N, R1 = (R0 + 17) / 2;
  tload(IC(R0), IC(R1));
  tfaddr();
#pragma unroll
  for (int k2 = 0; k2 < 8; ++k2) tfread(0, k2);
  SCHED_FENCE();
  tload(IC(R1), IC(16));
  ckaddr();
#pragma unroll
  for (int s = 0; s < 16; ++s) ckread(0, s);
  SCHED_FENCE();
  wg_barrier();
#pragma unroll
  for (int s = 0; s < 16; ++s) { PIN(ck[0][s]); PIN(tf[0][s >> 1][s & 1]); }
  SCHED_FENCE();
  TS(13);
  cluster(IC(16));
  twrite(1, 0, 17);
  qoff = WS_QP + (u32)((b3 * 8 + (w & 6) + hh) * 384 + 192 * (w & 1) + lane_id()) * 16;
#pragma unroll
  for (int k = 0; k < QN; ++k) qv[k] = ld_sc1(rs, qoff + 1024 * k);
  SCHED_FENCE();
  TS(14);
  wg_barrier();
  if (WIB) { const int lane = lane_id(); if (idx == 0 && w == 7 && lane == 0) st_sc1_32(rs, WS_EPOCH, tag); TS(11); TS_FLUSH(); asm volatile("s_endpgm" ::: "memory"); }
  auto so = [](int n) { return (n >> 2) + 4 * (n & 3); };
  ckaddr();
#pragma unroll
  for (int n = 0; n < CK1; ++n) ckread(1, so(n));
  u32 aq, amw, hco;
  {
    const int lane = lane_id(), r = lane >> 4, c16 = lane & 15;
    aq = L_Q + c16 * 1024 + ((r ^ c16) << 4);
    amw = L_MX + (w >> 2) * 256 + c16 * 16 + (w & 3) * 4;
    PIN(aq); PIN(amw);
    SCHED_FENCE();
    TS(15);
    const u32 qo[QN] = {qoff, qoff + 1024, qoff + 2048};
    row_wait<QN>(rs, qo, qv, tag, true, lane);
    SCHED_FENCE(); TS(16); SCHED_FENCE();
#pragma unroll
    for (int k = 0; k < QN; ++k) {
      *reinterpret_cast<LDSP u32x2*>((LDSP char*)pa64[k]) = u32x2{qv[k][0], qv[k][1]};
      *reinterpret_cast<LDSP u32*>((LDSP char*)pa32[k]) = qv[k][2];
    }
    SCHED_FENCE();
    wload(3);
    SCHED_FENCE();
    wg_barrier();
    SCHED_FENCE();
    TS(17);
  }
  u32x4 hc;
  SCHED_FENCE();
  {
    const u32 tsc = clk32();
    f32x4 sc[2], sd[2];
    u32x4 qa[16];
    u32 amx, apw, amr, apf[4], gb, gm, lane, r, c16;
    auto qread = [&](int n) INL { qa[n] = *reinterpret_cast<const LDSP u32x4*>((const LDSP char*)(aq ^ (u32)(64 * (n >> 2))) + (n & 3) * 256); };
#pragma unroll
    for (int n = 0; n < QA; ++n) qread(n);
#pragma unroll
    for (int n = 0; n < 16; ++n) {
      SCHED_FENCE();
      if (n + QA < 16) qread(n + QA);
      if (n + CK1 < 16) ckread(1, so(n + CK1));
      SCHED_FENCE();
#pragma unroll
      for (int kb = 0; kb < 2; ++kb) {
        if (n & 1) sd[kb] = mfma16(asbf(ck[kb][so(n)]), asbf(qa[n]), n > 1 ? sd[kb] : f32x4{0.f, 0.f, 0.f, 0.f});
        else sc[kb] = mfma16(asbf(ck[kb][so(n)]), asbf(qa[n]), n ? sc[kb] : f32x4{0.f, 0.f, 0.f, 0.f});
      }
      if (n == 0) st_sc1(rs, w == 7 && lane_id() == 0 ? WS_FLG + (u32)idx * 128 : OOB, u32x4{tag, tag, tsc, tag});
      if (n == 8) {
        lane = lane_id(); r = lane >> 4; c16 = lane & 15;
        amx = L_MX + c16 * 16;
        apw = L_P + c16 * 512 + (((2 * w + (r >> 1)) ^ c16) << 4) + 8 * (r & 1);
        PIN(amx); PIN(apw);
      }
      if (n == 9) { amr = L_MX + (c16 & 4) * 128 + (4 * r + (c16 & 2) + (c16 >> 3)) * 16; PIN(amr); }
      if (n == 10) {
        gb = WS_PART + (u32)((b3 * 16 + kp) * 8 + w) * PBLK; gm = gb + 4096 + hh * 256 + lane * 4;
        gb += hh * 2048 + r * 512 + c16 * 16;
        hco = WS_FLG + (u32)((w & 3) + 4 * (c16 & 1) + (c16 & 8) + 16 * ((w & 4) + ((c16 & 7) >> 1))) * 128;
        PIN(gb); PIN(gm); PIN(hco);
      }
      if (n == 11) {
#pragma unroll
        for (int k = 0; k < 4; ++k) { apf[k] = (L_P + c16 * 512 + ((r ^ c16) << 4)) ^ (u32)(k << 6); PIN(apf[k]); }
      }
      if (n == 12) {
        tfaddr();
#pragma unroll
        for (int k = 0; k < 8; ++k) PIN(at[k >> 1][k & 1]);
      }
    }
    SCHED_FENCE();
    {
      sc[0] += sd[0]; sc[1] += sd[1];
      *reinterpret_cast<LDSP float*>((LDSP char*)amw) = x64_maxq(max8q(sc[0], sc[1])) * SCL2;
    }
    SCHED_FENCE(); TS(4); SCHED_FENCE();
    wload(4);
    SCHED_FENCE();
    wg_barrier();
    SCHED_FENCE();
    {
      const f32x4 a0 = *reinterpret_cast<const LDSP f32x4*>((const LDSP char*)amx), a1 = *reinterpret_cast<const LDSP f32x4*>((const LDSP char*)amx + 256);
      SCHED_FENCE();
#pragma unroll
      for (int k2 = 0; k2 < TFS; ++k2) tfread(1, k2);
      SCHED_FENCE();
      const float m = max8q(a0, a1);
      const f32x4 x0 = sc[0], x1 = sc[1];
      u32x2 p0, p1;
      p0[0] = pk2(__builtin_amdgcn_exp2f(x0[0] * SCL2 - m), __builtin_amdgcn_exp2f(x0[1] * SCL2 - m));
      p0[1] = pk2(__builtin_amdgcn_exp2f(x0[2] * SCL2 - m), __builtin_amdgcn_exp2f(x0[3] * SCL2 - m));
      p1[0] = pk2(__builtin_amdgcn_exp2f(x1[0] * SCL2 - m), __builtin_amdgcn_exp2f(x1[1] * SCL2 - m));
      p1[1] = pk2(__builtin_amdgcn_exp2f(x1[2] * SCL2 - m), __builtin_amdgcn_exp2f(x1[3] * SCL2 - m));
      *reinterpret_cast<LDSP u32x2*>((LDSP char*)apw) = p0;
      *reinterpret_cast<LDSP u32x2*>((LDSP char*)apw + 256) = p1;
      SCHED_FENCE();
      const float ls = x32_sum(x16_sum(dot2(p0[1], 0x3f803f80u, dot2(p0[0], 0x3f803f80u, 0.f)) + dot2(p1[1], 0x3f803f80u, dot2(p1[0], 0x3f803f80u, 0.f))));
      *reinterpret_cast<LDSP float*>((LDSP char*)amw + 512) = ls;
    }
    wg_barrier();
    SCHED_FENCE(); TS(5); SCHED_FENCE();
    hc = ld_sc1(rs, hco);
    SCHED_FENCE();
    u32x4 pf[8];
    f32x4 m0, m1;
    auto pread = [&](int k) INL { pf[k] = *reinterpret_cast<const LDSP u32x4*>((const LDSP char*)apf[k & 3] + (k >> 2) * 256); };
#pragma unroll
    for (int k = 0; k < 4; ++k) { pread(k); SCHED_FENCE(); }
    wload(5);
    SCHED_FENCE();
    f32x4 acc[4];
    u32 cs[2], mw;
    auto pack = [&](int ip) INL {
      const u32 a0 = pk2(acc[0][2 * ip], acc[1][2 * ip]), a1 = pk2(acc[2][2 * ip], acc[3][2 * ip]);
      const u32 b0 = pk2(acc[0][2 * ip + 1], acc[1][2 * ip + 1]), b1 = pk2(acc[2][2 * ip + 1], acc[3][2 * ip + 1]);
      const u32x4 x = u32x4{dppb<0x128, 0xc>(a0, b0) + tag, dppb<0x128, 0xc>(a1, b1), dppb<0x128, 0x3>(b0, a0), dppb<0x128, 0x3>(b1, a1)};
      st_sc1(rs, gb + ip * 256, x);
      u32 c = (x[0] + x[1]) + (x[2] + x[3]);
      c += dppu<0xB1>(c); c += dppu<0x4E>(c);
      cs[ip] = c;
      PIN(cs[ip]);
    };
#pragma unroll
    for (int n = 0; n < 32; ++n) {
      acc[n & 3] = mfma16(asbf(pf[n >> 2]), tf[n >> 4][2 * ((n >> 2) & 3) + ((n >> 1) & 1)][n & 1], n < 4 ? f32x4{0.f, 0.f, 0.f, 0.f} : acc[n & 3]);
      SCHED_FENCE();
      if (n % 3 == 0 && n / 3 < 4) pread(4 + n / 3);
      if (n % 2 == 1 && TFS + n / 2 < 8) tfread(1, TFS + n / 2);
      if (n == 18) { m0 = *reinterpret_cast<const LDSP f32x4*>((const LDSP char*)amr); m1 = *reinterpret_cast<const LDSP f32x4*>((const LDSP char*)amr + 256); }
      if (n == 27) {
        PIN(m0); PIN(m1);
        const float mx = max8q(m0, m1);
        const float sm = ((m0[0] + m0[1]) + (m0[2] + m0[3])) + ((m1[0] + m1[1]) + (m1[2] + m1[3]));
        mw = __float_as_uint((c16 & 4) ? sm : mx);
        PIN(mw);
      }
      SCHED_FENCE();
    }
    SCHED_FENCE(); TS(6); SCHED_FENCE();
    pack(0); pack(1);
    st_sc1_32(rs, gm, mw + ((c16 & 1) ? ((c16 & 2) ? cs[1] : cs[0]) + tag : 0u));
    SCHED_FENCE();
  }
  const int lane = lane_id(), tid = w * 64 + lane, r = lane >> 4, c16 = lane & 15;
  s16x4 owf[8]; bf16x8 uvf[4];
  auto uvwrite = [&]() {
#pragma unroll
    for (int n = 0; n < 4; ++n) {
      const int c = 16 * w + 4 * r + n;
      *reinterpret_cast<u32x4*>(smem + L_T + w * 16384 + (4 * r + n) * 256 + ((c16 ^ (2 * (((c >> 4) & 3) | ((c & 1) << 2)))) << 4)) = uv[n];
    }
  };
  auto uvread = [&]() {
    const u32 tq = c16 >> 2, tp = lane & 3;
#pragma unroll
    for (int s = 0; s < 4; ++s) {
      const u32 c0 = 64 * (s >> 1) + 16 * tq + 4 * (s & 1) + r, sw = 2 * (tq | (4 * (r & 1)));
      const char* p = smem + L_T + (c0 >> 4) * 16384 + (c0 & 15) * 256 + (((2 * w + (tp >> 1)) ^ sw) << 4) + 8 * (tp & 1);
      s16x4 b0 = __builtin_amdgcn_ds_read_tr16_b64_v4i16((LDSP s16x4*)p);
      s16x4 b1 = __builtin_amdgcn_ds_read_tr16_b64_v4i16((LDSP s16x4*)(p + 8 * 256));
      uvf[s] = __builtin_shufflevector(b0, b1, 0, 1, 2, 3, 4, 5, 6, 7);
      if (UVP < 6) PIN(uvf[s]);
    }
  };
  auto owpack = [&]() {
#pragma unroll
    for (int j = 0; j < 8; ++j) { owf[j] = kfrag(ow, j); PIN(owf[j]); }
  };
  if (UVP < 6) uvwrite();
  wg_barrier();
  SCHED_FENCE();
  TS(20);
  wload(6);
  SCHED_FENCE();
  const u32 pblk = WS_PART + (u32)(((w & 3) * 16 + 8 * (w >> 2) + r) * 8 + 2 * e4 + (c16 >> 3)) * PBLK;
  u32 poff = pblk + h * 128 + ((c16 & 7) << 4), moff = pblk + (c16 & 1) * (32 * PBLK) + 4096 + (4 * (h >> 2) + 2 * (h & 1) + ((c16 >> 2) & 1)) * 16 + 4 * (h & 2);
  PIN(moff);
  u32 lrd = L_LAT + (c16 & 3) * 288 + 16 * r, mrd = L_RS + 16 * (c16 & 3);
  const u32 word = (tid >> 7) * 512 + 128 * e4 + (tid & 127);
  u32 cxa = WS_ACX + (u32)(h * 512 + tid) * 8, exw = L_EX + (w * 512 + ppos(r, c16)) * 4;
  u32 aoff = WS_ACC + 65536 + (word >> 3) * 256 + (word & 7) * 8, ooff = ((tid >> 7) * 1024 + 512 + 128 * e4 + pcol(tid & 127)) * 2;
  {
    u32 off[2]; u32x4 v[2]; u32x2 mv;
#pragma unroll
    for (int t = 0; t < 2; ++t) { off[t] = poff + (u32)(t * 32 * PBLK); PIN(off[t]); }
    both_halves(rs, hco, hc, tag);
    if (idx == 0 && w == 7) {
      const u32 eoff = WS_QX + (u32)((lane >> 2) * 16 + (lane & 3)) * DH * 8;
#pragma unroll 1
      for (u32 spins = 0; spins < SPIN_CAP; ++spins) {
        const u32x4 e0 = ld_sc1(rs, eoff), e1 = ld_sc1(rs, eoff + 16 * 16 * DH * 8);
        if (__all((e0[1] == tag) & (e0[3] == tag) & (e1[1] == tag) & (e1[3] == tag))) break;
        asm volatile("" ::: "memory");
      }
      if (lane == 0) st_sc1_32(rs, WS_EPOCH, tag);
    }
    hint_wait(rs, hco + 8, (c16 >> 3) == (h >> 4) ? u32x2{hc[2], hc[3]} : u32x2{clk32() - 1000u, tag}, tag, THC);
    TS(7);
    asm volatile("" ::: "memory");
    mv = ld_sc1_64(rs, moff);
    SCHED_FENCE();
    row_issue<2>(rs, off, v);
    SCHED_FENCE();
    wload(7);
    SCHED_FENCE();
    if (UVP < 6) uvread();
    if (UVP == 6) uvwrite();
    if (OWP < 7) owpack();
    u32 lad = L_LAT + w * 288 + c16 * 16 + 4 * r, rsa = L_RS + 16 * (w & 3) + 8 * (w >> 2) + (lane & 4);
    PIN(rsa); PIN(lrd); PIN(mrd);
    PIN(lad); PIN(ooff); PIN(aoff); PIN(cxa); PIN(exw);
    SCHED_FENCE();
    auto bad = [&]() {
      u32 S[2];
#pragma unroll
      for (int t = 0; t < 2; ++t) S[t] = (v[t][0] + v[t][1]) + (v[t][2] + v[t][3]);
      const bool o1 = lane & 1;
      const u32 u0 = (o1 ? S[1] : S[0]) + dppu<0xB1>(o1 ? S[0] : S[1]);
      const u32 s = u0 + dppu<0x4E>(u0);
      return __ballot(tag + mv[0] + s != mv[1]);
    };
    s16x4 cf; float ml;
    auto weights = [&]() {
      float M = __uint_as_float(mv[0]);
      asm volatile("s_nop 1\n\tv_max_f32_dpp %0, %0, %0 quad_perm:[1,0,3,2] row_mask:0xf bank_mask:0xf\n\ts_nop 1" : "+v"(M));
      M = x32_max(x16_max(M));
      const float ex = __builtin_amdgcn_exp2f(__uint_as_float(mv[0]) - M);
      u32 eb = pk2(ex, ex);
      eb = dppb<0x114, 0xa>(eb, eb);
      cf = __builtin_bit_cast(s16x4, u32x2{plo(dppu<0x55>(eb), dppu<0x00>(eb)), 0u});
      float ls = bflo(eb) * __uint_as_float(mv[0]);
      ls += dppf<0xB1>(ls);
      ls = x32_sum(x16_sum(ls));
      ml = (lane & 4) ? ls : M;
      PIN(cf); PIN(ml);
    };
    weights();
    SCHED_FENCE();
    u64 bm = bad();
    if (__builtin_expect(bm != 0ull, 0)) {
#pragma unroll 1
      for (u32 spins = 0; spins < SPIN_CAP; ++spins) {
        __builtin_amdgcn_s_sleep(2);
        asm volatile("" ::: "memory");
        const u32 q = (u32)(bm >> (lane & 60));
        u32x4 t4[2];
#pragma unroll
        for (int t = 0; t < 2; ++t) t4[t] = ld_sc1(rs, ((q >> t) & 1) ? off[t] : OOB);
        const bool rm = (q >> (lane & 3)) & 1;
        const u32x2 t2 = ld_sc1_64(rs, rm ? moff : OOB);
#pragma unroll
        for (int t = 0; t < 2; ++t) v[t] = ((q >> t) & 1) ? t4[t] : v[t];
        mv = rm ? t2 : mv;
        weights();
        bm = bad();
        if (bm == 0ull) break;
      }
    }
    SCHED_FENCE();
    TS(8);
#pragma unroll
    for (int t = 0; t < 2; ++t) v[t][0] -= tag;
    f32x4 d[8];
#pragma unroll
    for (int j = 0; j < 8; ++j) d[j] = __builtin_amdgcn_mfma_f32_16x16x16bf16_1k(cf, __builtin_bit_cast(s16x4, u32x2{(j & 1) ? phi(v[1][j >> 1], v[0][j >> 1]) : plo(v[1][j >> 1], v[0][j >> 1]), 0u}), f32x4{0.f, 0.f, 0.f, 0.f}, 0, 0, 0);
    if ((lane & ~4) == 0) *reinterpret_cast<LDSP float*>((LDSP char*)rsa) = ml;
    SCHED_FENCE();
    wload(8);
    SCHED_FENCE();
#pragma unroll
    for (int j = 0; j < 8; ++j) PIN(d[j]);
    const float x0 = (r & 2) ? ((r & 1) ? d[6][0] : d[4][0]) : ((r & 1) ? d[2][0] : d[0][0]), x1 = (r & 2) ? ((r & 1) ? d[7][0] : d[5][0]) : ((r & 1) ? d[3][0] : d[1][0]);
    *reinterpret_cast<LDSP u32*>((LDSP char*)lad) = pk2(x0, x1);
    if (UVP == 7) uvwrite();
  }
  wg_barrier();
  SCHED_FENCE();
  TS(22);
  {
    if (UVP >= 6) uvread();
    f32x4 c0 = f32x4{0.f, 0.f, 0.f, 0.f}, c1 = c0;
    const f32x4 ml = *reinterpret_cast<const LDSP f32x4*>((const LDSP char*)mrd);
    SCHED_FENCE();
    bf16x8 lf[2][4];
#pragma unroll
    for (int s = 0; s < 4; ++s) { lf[0][s] = *reinterpret_cast<const LDSP bf16x8*>((const LDSP char*)lrd + 64 * s); lf[1][s] = *reinterpret_cast<const LDSP bf16x8*>((const LDSP char*)lrd + 1152 + 64 * s); }
    SCHED_FENCE();
#pragma unroll
    for (int s = 0; s < 4; ++s) { c0 = mfma16(uvf[s], lf[0][s], c0); c1 = mfma16(uvf[s], lf[1][s], c1); }
    const float M = fmaxf(ml[0], ml[2]), a0 = __builtin_amdgcn_exp2f(ml[0] - M), a1 = __builtin_amdgcn_exp2f(ml[2] - M);
    const float rsum = __builtin_amdgcn_rcpf(fmaf(a0, ml[1], a1 * ml[3]));
    const bool i1 = c16 & 4, i2 = c16 & 8;
    float y0 = i1 ? c0[1] : c0[0], y1 = i1 ? c0[3] : c0[2], z0 = i1 ? c1[1] : c1[0], z1 = i1 ? c1[3] : c1[2];
    PIN(y0); PIN(y1); PIN(z0); PIN(z1);
    const float mine = fmaf(a0 * rsum, i2 ? y1 : y0, (a1 * rsum) * (i2 ? z1 : z0));
    TS(9);
    u64 t = fx_add(a.ws, rs, mine, cxa, WS_XC + (u32)((h * 4 + e4) * 512 + tid) * 16, tag);
    if (OWP >= 7) owpack();
#pragma unroll 1
    for (u32 spins = 0; spins < SPIN_CAP; ++spins) {
      if (__all((u32)(t >> 58) == 4u)) break;
      u64 z = 0; PIN(z);
      asm volatile("" ::: "memory");
      if (__builtin_expect(spins < 6, 1)) t = __hip_atomic_load(reinterpret_cast<u64*>(a.ws + cxa), __ATOMIC_RELAXED, __HIP_MEMORY_SCOPE_AGENT);
      else t = __hip_atomic_fetch_add(reinterpret_cast<u64*>(a.ws + cxa), z, __ATOMIC_RELAXED, __HIP_MEMORY_SCOPE_AGENT);
    }
    TS(12);
    const float x = fx_val<4, 8192>(rs, t, WS_XC + (u32)(h * 2048 + tid) * 16, tag);
    const u32 xb = __float_as_uint(x), pa = dppb<0x124, 0xa>(dppb<0x12c, 0x5>(0u, xb), xb);
    const u32 pk = (c16 & 4) ? pk2(__uint_as_float(pa), x) : pk2(x, __uint_as_float(pa));
    const u32 alo = dppb<0x128, 0xc>(pk, pk), ahi = dppb<0x128, 0x3>(pk, pk);
    f32x4 s0 = f32x4{0.f, 0.f, 0.f, 0.f}, s1 = s0;
#pragma unroll
    for (int q = 0; q < 4; ++q) {
      const bool on = (c16 >> 2) == q;
      const s16x4 av = __builtin_bit_cast(s16x4, u32x2{on ? alo : 0u, on ? ahi : 0u});
      s0 = __builtin_amdgcn_mfma_f32_16x16x16bf16_1k(av, owf[2 * q], s0, 0, 0, 0);
      s1 = __builtin_amdgcn_mfma_f32_16x16x16bf16_1k(av, owf[2 * q + 1], s1, 0, 0, 0);
    }
#pragma unroll
    for (int b = 0; b < NB; ++b) *reinterpret_cast<LDSP u32x2*>((LDSP char*)exw + b * 512) = u32x2{__float_as_uint(s0[b]), __float_as_uint(s1[b])};
  }
  wg_barrier();
  {
    const float* pp = reinterpret_cast<const float*>(smem + L_EX) + tid;
    const float y = ((pp[0] + pp[512]) + (pp[1024] + pp[1536])) + ((pp[2048] + pp[2560]) + (pp[3072] + pp[3584]));
    TS(10);
    const u64 t = fx_add(a.ws, rs, y, aoff, WS_XB + h * 32768 + word * 16, tag);
    if ((u32)(t >> 58) == 32u) { const float s = fx_val<32, 32768>(rs, t, WS_XB + word * 16, tag); *reinterpret_cast<u16*>(reinterpret_cast<char*>(a.out) + ooff) = (u16)pk2(s, s); }
  }
  TS(11);
  TS_FLUSH();
}

constexpr int upto(int ch) { const int y = PY + CU * (ch + 1), x = 4 * (ch + AW + 1), z = ch < 0 ? PY : (x > y ? x : y); return z < 4 * NCH ? z : 4 * NCH; }
DI void roleA(const KArgs& a, char* smem, const int g, const int idx, const int w, const int lane, const u32x4 (&R)[4], const u32 kvo, const u32 cpf, u32 t0, const u64 traw) {
  TS_DECL; TSRAW;
  const int r = lane >> 4, c16 = lane & 15, tid = w * 64 + lane;
  const int h = 4 * (idx & 7) + ((idx >> 3) & 3), b = idx >> 5;
  const __amdgpu_buffer_rsrc_t rs = __builtin_amdgcn_make_buffer_rsrc((void*)a.ws, 0, (int)WS_END, RSRC_W3);
  u32 tag = *reinterpret_cast<const u32*>(a.ws + WS_EPOCH);
  u32x4 kv[16][4];
  u32x4 ov[16];
  const u16* wop = a.woa + (size_t)(h * DH + 64 * (c16 >> 3) + 8 * r) * DM + 64 * w + 8 * (c16 & 7);
  auto woload = [&](int n) INL { if (n < WIO) { ov[n] = u32x4{1u, 2u, 3u, 4u}; PIN(ov[n]); } else ov[n] = ldsh(wop + (size_t)(qoffs(n >> 2, b) + (n & 3)) * DM); };
  auto kvissue = [&](auto lo, auto hi) INL {
    unroll_c<hi - lo>([&](auto n0) INL {
      constexpr int n = lo + n0;
      constexpr int ch = n >> 2, isv = ch < C0 ? ch & 1 : ch >= C0 + ND, st = ch < C0 ? ch >> 1 : 8 - ND + (ch - C0) % ND;
      if constexpr (n < PN3) pace<(n < PN1 ? PT0 + n * PD1 : n < PN2 ? PT0 + PN1 * PD1 + (n - PN1) * PD2 : PT0 + PN1 * PD1 + (PN2 - PN1) * PD2 + (n - PN2) * PD3)>(t0);
      kv[ch][n & 3] = ldnt(reinterpret_cast<const u16*>(reinterpret_cast<const char*>(isv ? a.vc : a.kc) + (size_t)((((st + (h >> 2)) & 7) ^ (KM ? h & 2 : 0)) * 4096) + 4 * (n & 3) * (DH * 2) + kvo));
      if constexpr (CO && n == CON) { SCHED_FENCE(); t0 = early_start(a.ws, traw, t0); SCHED_FENCE(); }
      unroll_c<(WP0 ? 4 : 8)>([&](auto k) INL { if constexpr (WK0 + k * WKD / 2 == n) { SCHED_FENCE(); woload(k + (WP0 ? 4 : 0)); SCHED_FENCE(); } });
    });
  };
  {
    const int m0 = 128 * b + 16 * w + 4 * r;
    const u32x2 xf = *reinterpret_cast<const u32x2*>(a.xa + (c16 & 3) * DM + m0);
    SCHED_FENCE();
    if (WP0) { for (int k = 0; k < 4; ++k) woload(k); SCHED_FENCE(); }
    __builtin_amdgcn_s_barrier();
    SCHED_FENCE();
    kvissue(IC(0), IC(A0));
    SCHED_FENCE();
    asm volatile("" :: "v"(cpf));
    PIN(tag); tag = next_tag(tag);
    f32x4 ac[8];
#pragma unroll
    for (int j = 0; j < 8; ++j) ac[j] = __builtin_amdgcn_mfma_f32_16x16x16bf16_1k(__builtin_bit_cast(s16x4, xf), kfrag(R, j), f32x4{0.f, 0.f, 0.f, 0.f}, 0, 0, 0);
    const f32x4 s0 = sel4(ac[0], ac[2], ac[4], ac[6], r), s1 = sel4(ac[1], ac[3], ac[5], ac[7], r);
    float* pp = reinterpret_cast<float*>(smem + L_AX) + w * 512 + ppos(r, c16);
#pragma unroll
    for (int k = 0; k < NB; ++k) *reinterpret_cast<u32x2*>(pp + k * DH) = u32x2{__float_as_uint(s0[k]), __float_as_uint(s1[k])};
  }
  wg_barrier();
  if (w < 4) {
    const float* pp = reinterpret_cast<const float*>(smem + L_AX) + 2 * tid;
    f32x2_ t[8];
#pragma unroll
    for (int k = 0; k < 8; ++k) t[k] = *reinterpret_cast<const f32x2_*>(pp + 512 * k);
    const f32x2_ s = ((t[0] + t[1]) + (t[2] + t[3])) + ((t[4] + t[5]) + (t[6] + t[7]));
    st_sc1(rs, WS_QX + (u32)(((h * 4 + w) * 4 + b) * DH + 2 * lane) * 8, u32x4{__float_as_uint(s[0]), tag, __float_as_uint(s[1]), tag});
  }
  SCHED_FENCE();
  TS(21);
  __builtin_amdgcn_s_barrier();
  SCHED_FENCE();
  if (NOA) { TS_FLUSH(); return; }
  kvissue(IC(A0), IC(PX));
  SCHED_FENCE();
  u32x4 qpk;
  {
    const u32 off = WS_QX + (u32)(((h * 4 + b) * 4 + ((lane >> 3) & 3)) * DH + 16 * w + 2 * (lane & 7)) * 8;
    u32x4 v = ld_sc1(rs, off);
    SCHED_FENCE();
    kvissue(IC(PX), IC(PY));
    SCHED_FENCE();
    q_finish(rs, off, v, tag, smem + L_AQ + pcol(16 * w + 2 * (lane & 7)) * 2, lane);
    wg_barrier();
    qpk = *reinterpret_cast<const u32x4*>(smem + L_AQ + 16 * c16);
  }
  TS(1);
  SCHED_FENCE();
  float m = -INFINITY, l = 0.f, sd[ND];
  f32x4 acc[8];
  s16x4 pa[ND];
#pragma unroll
  for (int k = 0; k < 8; ++k) acc[k] = f32x4{0.f, 0.f, 0.f, 0.f};
  u32x4 wf[2][8];
  auto wpack = [&](int q) {
#pragma unroll
    for (int j = 0; j < 8; ++j) {
      const u32x2 t = __builtin_bit_cast(u32x2, kfrag(ov + 4 * q, j));
      wf[q >> 1][j][2 * (q & 1)] = t[0]; wf[q >> 1][j][2 * (q & 1) + 1] = t[1];
      PIN(wf[q >> 1][j]);
    }
  };
  const bool hi8 = c16 & 8, hi4 = c16 & 4;
  u32 aaf[4], aml = L_AST + 2304 + 8 * (lane & 7), asw = L_AST + w * 272 + 16 * c16;
#pragma unroll
  for (int q = 0; q < 4; ++q) { aaf[q] = L_AST + (c16 & 7) * 272 + (64 * (c16 >> 3) + 8 * r + qoffs(q, b)) * 2; PIN(aaf[q]); }
  u32 aoff = WS_ACC + b * 16384 + (tid >> 3) * 256 + (tid & 7) * 8, ooff = (b * 1024 + 64 * w + 8 * (c16 & 7) + 2 * r + (c16 >> 3)) * 2;
  PIN(aml); PIN(asw); PIN(aoff); PIN(ooff);
  const u32 foff = WS_FLG + (u32)(idx ^ (lane & 8)) * 128;
  u32x2 fl = u32x2{tag, tag};
  auto wit = [](int n) { return n < 8 ? -1 : n < 12 ? WS1 : WS2; };
  auto wdn = [&](int n) { return WP0 && n < 4 ? 1 : (WK0 + (n - (WP0 ? 4 : 0)) * WKD / 2) / 4 + 2; };
  auto steps = [&](auto lo, auto hi) INL {
  unroll_c<hi - lo>([&](auto ch0) INL {
    constexpr int ch = lo + ch0;
    SCHED_FENCE();
    kvissue(IC(upto(ch - 1)), IC(upto(ch)));
#pragma unroll
    for (int n = 0; n < 16; ++n)
      if (wit(n) == ch) woload(n);
    if (ch == FLI && w == FLW) fl = __builtin_amdgcn_raw_buffer_load_b64(rs, foff, 0, 16);
    SCHED_FENCE();
    if (ASYNC > 0 && ch % ASYNC == ASYNC - 1 && ch != C0 + ND - 1 && ch < BVE) { __builtin_amdgcn_s_barrier(); SCHED_FENCE(); }
    if constexpr (ch < C0 && !(ch & 1)) {
      const float s = score4(kv[ch], qpk, hi8, hi4);
      if (__builtin_expect(__any(s > m), ch < 2)) {
        float mn = fmaxf(fmaxf(s, dppf<0x128>(s)), m);
        mn = x32_max(x16_max(fmaxf(mn, dppf<0x124>(mn))));
        const float alpha = __builtin_amdgcn_exp2f(m - mn);
        m = mn;
        l *= alpha;
#pragma unroll
        for (int k = 0; k < 8; ++k) acc[k][0] *= alpha;
      }
      const float p = __builtin_amdgcn_exp2f(s - m);
      l += p;
      pa[0] = ppack(p);
      PIN(pa[0]); PIN(l);
    } else if constexpr (ch >= C0 && ch < C0 + ND - 1) {
      sd[ch - C0] = score4(kv[ch], qpk, hi8, hi4);
      PIN(sd[ch - C0]);
    } else if constexpr (ch == C0 + ND - 1) {
      sd[ND - 1] = score4(kv[ch], qpk, hi8, hi4);
      float mn = sd[0];
#pragma unroll
      for (int k = 1; k < ND; ++k) mn = fmaxf(mn, sd[k]);
      mn = fmaxf(fmaxf(mn, dppf<0x128>(mn)), m);
      mn = x32_max(x16_max(fmaxf(mn, dppf<0x124>(mn))));
      const float beta = __builtin_amdgcn_exp2f(m - mn);
      l *= beta;
#pragma unroll
      for (int k = 0; k < ND; ++k) { sd[k] = __builtin_amdgcn_exp2f(sd[k] - mn); l += sd[k]; }
      l += dppf<0x128>(l); l += dppf<0x124>(l);
      l = x32_sum(x16_sum(l));
      if (lane == 0) *reinterpret_cast<f32x2_*>(smem + L_AST + 2304 + 8 * w) = f32x2_{mn, l};
      wg_barrier();
      const f32x2_ ml = *reinterpret_cast<const LDSP f32x2_*>((const LDSP char*)aml);
      float M = fmaxf(ml[0], dppf<0xB1>(ml[0]));
      M = fmaxf(M, dppf<0x4E>(M)); M = fmaxf(M, dppf<0x141>(M));
      float L = __builtin_amdgcn_exp2f(ml[0] - M) * ml[1];
      L += dppf<0xB1>(L); L += dppf<0x4E>(L); L += dppf<0x141>(L);
      const float al = __builtin_amdgcn_exp2f(mn - M) * __builtin_amdgcn_rcpf(L), ab = al * beta;
#pragma unroll
      for (int k = 0; k < 8; ++k) { acc[k][0] *= ab; PIN(acc[k]); }
#pragma unroll
      for (int k = 0; k < ND; ++k) { pa[k] = ppack(sd[k] * al); PIN(pa[k]); }
    } else {
#pragma unroll
      for (int j = 0; j < 8; ++j) acc[j] = __builtin_amdgcn_mfma_f32_16x16x16bf16_1k(pa[ch < C0 ? 0 : ch - C0 - ND], kfrag(kv[ch], j), acc[j], 0, 0, 0);
#pragma unroll
      for (int j = 0; j < 8; ++j) PIN(acc[j]);
    }
#pragma unroll
    for (int q = 0; q < 2; ++q)
      if (wdn(4 * q + 3) - 1 == ch) wpack(q);
  });
  };
  steps(IC(0), IC(NCH - 1));
  if (!WIB && __builtin_expect(!__all((fl[0] == tag) & (fl[1] == tag)), 0)) {
#pragma unroll 1
    for (u32 spins = 0; spins < SPIN_CAP; ++spins) {
      asm volatile("" ::: "memory");
      fl = __builtin_amdgcn_raw_buffer_load_b64(rs, foff, 0, 16);
      if (__all((fl[0] == tag) & (fl[1] == tag))) break;
      __builtin_amdgcn_s_sleep(2);
    }
  }
  steps(IC(NCH - 1), IC(NCH));
  TS(2);
  if (lane < 16) *reinterpret_cast<LDSP u32x4*>((LDSP char*)asw) = u32x4{pk2(acc[0][0], acc[1][0]), pk2(acc[2][0], acc[3][0]), pk2(acc[4][0], acc[5][0]), pk2(acc[6][0], acc[7][0])};
  SCHED_FENCE();
#pragma unroll
  for (int n = 0; n < 16; ++n)
    if (wit(n) == NCH) woload(n);
  SCHED_FENCE();
  wg_barrier();
  TS(5);
  {
    u32x2 af[4];
#pragma unroll
    for (int q = 0; q < 4; ++q) af[q] = *reinterpret_cast<const LDSP u32x2*>((const LDSP char*)aaf[q]);
    SCHED_FENCE();
    f32x4 D[8];
    auto JO = [](int j) { return (j >> 1) + 4 * (j & 1); };
#pragma unroll
    for (int j = 0; j < 8; ++j) {
      D[j] = mfma16(asbf(u32x4{af[0][0], af[0][1], af[1][0], af[1][1]}), asbf(wf[0][j]), f32x4{0.f, 0.f, 0.f, 0.f});
      SCHED_FENCE();
      wf[1][JO(j)] = __builtin_bit_cast(u32x2, kfrag(ov + 8, JO(j))).xyxy;
      PIN(wf[1][JO(j)][0]); PIN(wf[1][JO(j)][1]);
      SCHED_FENCE();
    }
#pragma unroll
    for (int j = 0; j < 8; ++j) {
      const u32x2 t = __builtin_bit_cast(u32x2, kfrag(ov + 12, JO(j)));
      wf[1][JO(j)][2] = t[0]; wf[1][JO(j)][3] = t[1];
      D[JO(j)] = mfma16(asbf(u32x4{af[2][0], af[2][1], af[3][0], af[3][1]}), asbf(wf[1][JO(j)]), D[JO(j)]);
      SCHED_FENCE();
    }
    float y[8], u[4], v[2];
#pragma unroll
    for (int j = 0; j < 8; ++j) { const f32x2_ t = __builtin_shufflevector(D[JO(j)], D[JO(j)], 0, 1) + __builtin_shufflevector(D[JO(j)], D[JO(j)], 2, 3); y[JO(j)] = t[0] + t[1]; }
#pragma unroll
    for (int k = 0; k < 4; ++k) { auto x = __builtin_amdgcn_permlane32_swap(__float_as_uint(y[k]), __float_as_uint(y[k + 4]), false, false); u[k] = __uint_as_float(hi8 ? x[1] : x[0]); }
#pragma unroll
    for (int k = 0; k < 2; ++k) { auto x = __builtin_amdgcn_permlane16_swap(__float_as_uint(u[k]), __float_as_uint(u[k + 2]), false, false); v[k] = __uint_as_float(x[0]) + __uint_as_float(x[1]); }
    const float yy = (hi8 ? v[1] : v[0]) + dppf<0x128>(hi8 ? v[0] : v[1]);
    TS(3);
    u64 t = fx_add(a.ws, rs, yy, aoff, WS_XA + h * 32768 + (b * 512 + tid) * 16, tag);
    if ((u32)(t >> 58) == 32u) { const float s = fx_val<32, 32768>(rs, t, WS_XA + (b * 512 + tid) * 16, tag); *reinterpret_cast<u16*>(reinterpret_cast<char*>(a.out) + ooff) = (u16)pk2(s, s); }
  }
  TS(11);
  TS_FLUSH();
  asm volatile("s_endpgm" ::: "memory");
}

__global__ void __launch_bounds__(512, 2) fused(KArgs a) {
  extern __shared__ __attribute__((aligned(16))) char smem[];
  const int tid = threadIdx.x, lane = tid & 63, w = __builtin_amdgcn_readfirstlane(tid >> 6), g = blockIdx.x;
  const int idx = (g & 7) | ((g >> 4) << 3);
  const u64 traw = __builtin_amdgcn_s_memrealtime();
  const u32 t0 = (u32)traw - ((g & 4) ? ((g & 8) ? SKB : SKA) : 0);
  u32 cpf = 0;
  if (w >= 8 - CPF && (idx >> 3) < CPN) {
    u64 pc; asm volatile("s_getpc_b64 %0" : "=s"(pc));
    cpf = *reinterpret_cast<const __attribute__((address_space(1))) u32*>((pc & ~127ull) + min((7 - w) * 64 + lane, CPL) * 128);
  }
  SCHED_FENCE();
  u32 o[4], o2;
  {
    const u32 h = 4 * (idx & 7) + ((idx >> 3) & 3), b = idx >> 5, r = lane >> 4;
#pragma unroll
    for (int n = 0; n < 4; ++n) { o[n] = (128 * b + 16 * w + 4 * r + n) * (NH * DH * 2) + h * (DH * 2) + (lane & 15) * 16; PIN(o[n]); }
    u32 ob = (((idx & 3) * SK + ((idx >> 4) + (idx & 8) + 4 * (idx & 4)) * 128 + w) * DC + 8 * lane) * 2, oa = (((b * NH + h) * SK + (SK - WIN) + 128 * w + r) * DH + 8 * (lane & 15)) * 2;
    if (TM) ob = (((idx & 3) * SK + (((idx >> 5) & 1) + ((idx & 4) >> 1) + ((idx >> 2) & 4) + (idx & 8) + ((idx >> 6) << 4)) * 128 + (w & 3) + 16 * (w & 4)) * DC + 8 * lane) * 2;
    PIN(oa); PIN(ob);
    o2 = ((g >> 3) & 1) ? ob : oa;
    PIN(o2);
  }
  SCHED_FENCE();
  asm volatile("" :: "s"(a.xa), "s"(a.kc), "s"(a.vc), "s"(a.wqa), "s"(a.woa), "s"(a.xb), "s"(a.ckv), "s"(a.wqb), "s"(a.wuk), "s"(a.wuv), "s"(a.wob), "s"(a.ws), "s"(a.out), "s"(traw));
  u32x4 R[4];
  if (CO && g < 8 && tid == 0) __hip_atomic_store(reinterpret_cast<u64*>(a.ws + 2048 + 8 * g), traw, __ATOMIC_RELAXED, __HIP_MEMORY_SCOPE_AGENT);
  SCHED_FENCE();
  {
    const char* p = reinterpret_cast<const char*>(((g >> 3) & 1) ? a.wuk : a.wqa);
#pragma unroll
    for (int n = 0; n < 4; ++n) R[n] = ldnt(reinterpret_cast<const u16*>(p + o[n]));
    SCHED_FENCE();
  }
  if (__builtin_expect(!((g >> 3) & 1), 1)) roleA(a, smem, g, idx, w, lane, R, o2, cpf, t0, traw);
  else roleB(a, smem, g, idx, w, lane, R, o2, cpf, t0, traw);
}

extern "C" void kernel_launch(void* const* d_in, const int* in_sizes, int n_in,
                              void* d_out, int out_size, void* d_ws, size_t ws_size,
                              hipStream_t stream) {
  KArgs a;
  a.xa  = (const u16*)d_in[0];
  a.kc  = (const u16*)d_in[1];
  a.vc  = (const u16*)d_in[2];
  a.wqa = (const u16*)d_in[3];
  a.woa = (const u16*)d_in[4];
  a.xb  = (const u16*)d_in[5];
  a.ckv = (const u16*)d_in[6];
  a.wqb = (const u16*)d_in[7];
  a.wuk = (const u16*)d_in[8];
  a.wuv = (const u16*)d_in[9];
  a.wob = (const u16*)d_in[10];
  {
    const size_t need = (size_t)WS_END + (1u << 20);
    const size_t off = ws_size > need ? (((ws_size - need) / 8 * WOFF) & ~(size_t)0x1fffff) : 0;
    a.ws = (char*)d_ws + off;
  }
  a.out = (u16*)d_out;
  (void)hipFuncSetAttribute((const void*)fused, hipFuncAttributeMaxDynamicSharedMemorySize, L_END);
  fused<<<256, 512, L_END, stream>>>(a);
}
```

```cpp
#pragma clang diagnostic ignored "-Wint-to-pointer-cast"
#include <hip/hip_runtime.h>
#include <hip/hip_bf16.h>
#include <stdint.h>
#include <utility>

typedef unsigned int u32;
typedef unsigned short u16;
typedef unsigned long long u64;
using u32x4  = __attribute__((ext_vector_type(4))) unsigned;
using u32x2  = __attribute__((ext_vector_type(2))) unsigned;
using f32x4  = __attribute__((ext_vector_type(4))) float;
using bf16x8 = __attribute__((ext_vector_type(8))) short;
using s16x4  = __attribute__((ext_vector_type(4))) short;
#define DI __device__ __forceinline__
#define LDSP __attribute__((address_space(3)))
#define SCHED_FENCE() __builtin_amdgcn_sched_barrier(0)
#define PIN(x) asm volatile("" : "+v"(x))

DI float bflo(u32 v) { return __uint_as_float(v << 16); }
using f32x2_ = __attribute__((ext_vector_type(2))) float;
using bf16x2_ = __attribute__((ext_vector_type(2))) __bf16;
DI u32 pk2(float lo, float hi) { return __builtin_bit_cast(u32, __builtin_convertvector(f32x2_{lo, hi}, bf16x2_)); }
template <int CTRL> DI float dppf(float v) { return __builtin_bit_cast(float, __builtin_amdgcn_update_dpp(0, __builtin_bit_cast(int, v), CTRL, 0xf, 0xf, true)); }
template <int CTRL> DI u32 dppu(u32 v) { return (u32)__builtin_amdgcn_update_dpp(0, (int)v, CTRL, 0xf, 0xf, true); }
DI float row16_maxq(float v) {
  asm volatile("s_nop 1\n\tv_max_f32_dpp %0, %0, %0 row_ror:8 row_mask:0xf bank_mask:0xf\n\ts_nop 1\n\tv_max_f32_dpp %0, %0, %0 row_ror:4 row_mask:0xf bank_mask:0xf\n\t"
               "s_nop 1\n\tv_max_f32_dpp %0, %0, %0 row_ror:2 row_mask:0xf bank_mask:0xf\n\ts_nop 1\n\tv_max_f32_dpp %0, %0, %0 row_ror:1 row_mask:0xf bank_mask:0xf\n\ts_nop 1" : "+v"(v));
  return v;
}
DI float max3q(float a, float b, float c) { float d; asm("v_max3_f32 %0, %1, %2, %3" : "=v"(d) : "v"(a), "v"(b), "v"(c)); return d; }
DI float max8q(f32x4 a, f32x4 b) { return max3q(max3q(a[0], a[1], a[2]), max3q(a[3], b[0], b[1]), max3q(b[2], b[3], b[3])); }
DI float x64_maxq(float v) {
  auto s = __builtin_amdgcn_permlane16_swap(__float_as_uint(v), __float_as_uint(v), false, false);
  v = max3q(__uint_as_float(s[0]), __uint_as_float(s[1]), __uint_as_float(s[1]));
  auto t = __builtin_amdgcn_permlane32_swap(__float_as_uint(v), __float_as_uint(v), false, false);
  return max3q(__uint_as_float(t[0]), __uint_as_float(t[1]), __uint_as_float(t[1]));
}
DI float x16_sum(float v) { auto s = __builtin_amdgcn_permlane16_swap(__float_as_uint(v), __float_as_uint(v), false, false); return __uint_as_float(s[0]) + __uint_as_float(s[1]); }
DI float x32_sum(float v) { auto s = __builtin_amdgcn_permlane32_swap(__float_as_uint(v), __float_as_uint(v), false, false); return __uint_as_float(s[0]) + __uint_as_float(s[1]); }
DI float x16_max(float v) { auto s = __builtin_amdgcn_permlane16_swap(__float_as_uint(v), __float_as_uint(v), false, false); return fmaxf(__uint_as_float(s[0]), __uint_as_float(s[1])); }
DI float x32_max(float v) { auto s = __builtin_amdgcn_permlane32_swap(__float_as_uint(v), __float_as_uint(v), false, false); return fmaxf(__uint_as_float(s[0]), __uint_as_float(s[1])); }
using bf16x2 = __attribute__((ext_vector_type(2))) __bf16;
DI float dot2(u32 a, u32 b, float c) { return __builtin_amdgcn_fdot2_f32_bf16(__builtin_bit_cast(bf16x2, a), __builtin_bit_cast(bf16x2, b), c, false); }
DI u32 plo(u32 r1, u32 r0) { return __builtin_amdgcn_perm(r1, r0, 0x05040100u); }
DI u32 phi(u32 r1, u32 r0) { return __builtin_amdgcn_perm(r1, r0, 0x07060302u); }
DI bf16x8 bfrag(const u32x4* R, int j) {
  const int q = j >> 1;
  u32x4 o;
  if (j & 1) { o[0] = phi(R[1][q], R[0][q]); o[1] = phi(R[3][q], R[2][q]); o[2] = phi(R[5][q], R[4][q]); o[3] = phi(R[7][q], R[6][q]); }
  else       { o[0] = plo(R[1][q], R[0][q]); o[1] = plo(R[3][q], R[2][q]); o[2] = plo(R[5][q], R[4][q]); o[3] = plo(R[7][q], R[6][q]); }
  return __builtin_bit_cast(bf16x8, o);
}
DI s16x4 kfrag(const u32x4* R, int j) {
  const int q = j >> 1;
  u32x2 o;
  if (j & 1) { o[0] = phi(R[1][q], R[0][q]); o[1] = phi(R[3][q], R[2][q]); }
  else       { o[0] = plo(R[1][q], R[0][q]); o[1] = plo(R[3][q], R[2][q]); }
  return __builtin_bit_cast(s16x4, o);
}
DI f32x4 sel4(f32x4 a, f32x4 b, f32x4 c, f32x4 d, int r) { f32x4 lo = (r & 1) ? b : a, hi = (r & 1) ? d : c; return (r & 2) ? hi : lo; }
DI f32x4 mfma16(bf16x8 a, bf16x8 b, f32x4 c) { return __builtin_amdgcn_mfma_f32_16x16x32_bf16(a, b, c, 0, 0, 0); }
DI bf16x8 asbf(u32x4 v) { return __builtin_bit_cast(bf16x8, v); }
DI float dot8(const u32x4 kv, const u32x4 q) {
  float s = dot2(kv[0], q[0], 0.f);
  s = dot2(kv[1], q[1], s); s = dot2(kv[2], q[2], s); s = dot2(kv[3], q[3], s);
  return s;
}
constexpr int NB = 4, NH = 32, DH = 128, SK = 4096, WIN = 1024, DM = 512, DC = 512;
constexpr float LOG2E = 1.4426950408889634f;
constexpr float SCL2 = 0.088388347648318440f * LOG2E;

constexpr int WOFF = 8;
constexpr int AW = 3;
constexpr int ASYNC = 1;
constexpr int A0 = 4;
constexpr int PX = 14;
constexpr int PY = 22;
constexpr int CU = 2;
constexpr int FLI = 10;
constexpr int FLW = 0;
constexpr int UVP = 3;
constexpr int OWP = 8;
constexpr int CPF = 3;
constexpr int CPN = 1;
constexpr int CPL = 191;
constexpr int BVE = 13;
constexpr int THC = 325;
constexpr int PT0 = 900;
constexpr int PD1 = 270;
constexpr int PN1 = 12;
constexpr int PD2 = 248;
constexpr int PN2 = 38;
constexpr int BT0 = 900;
constexpr int BD1 = 425;
constexpr int BDQ = 60;
constexpr int BD2 = 375;
constexpr int XR = 4;
constexpr int UKR = 5;
constexpr int X0 = 8;
constexpr int TI = 3;
constexpr int T2N = 1;
constexpr int TH = 13;
constexpr int T4N = 1;
constexpr int WLG = 3;
constexpr int CK1 = 8;
constexpr int QA = 3;
constexpr int TFS = 4;
constexpr int WK0 = 17;
constexpr int WKD = 7;
constexpr int sibs_[17] = {0, 0, 0, 0, 4, 4, 4, 4, 8, 8, 8, 8, 8, 8, 8, 8, 16};
constexpr int sb(int i) { return i < 0 ? 0 : sibs_[i]; }
constexpr int NCH = 16;
constexpr u32 WS_EPOCH = 0;
constexpr u32 WS_QP    = 4096;
constexpr u32 PBLK     = 4608;
constexpr u32 WS_PART  = WS_QP + NB * 8 * 384 * 16;
constexpr u32 WS_QX    = WS_PART + NB * 16 * 8 * PBLK;
constexpr u32 WS_ACC   = WS_QX + NH * NB * 4 * DH * 8;
constexpr u32 WS_ACX   = WS_ACC + 2 * 65536;
constexpr u32 WS_FLG   = WS_ACX + 32 * 4096;
constexpr u32 WS_XA    = WS_FLG + 128 * 128;
constexpr u32 WS_XB    = WS_XA + 32 * 2048 * 16;
constexpr u32 WS_XC    = WS_XB + 32 * 2048 * 16;
constexpr u32 WS_END   = WS_XC + 32 * 2048 * 16;
constexpr u32 RSRC_W3  = 0x00020000;
constexpr u32 SPIN_CAP = 1u << 17;

constexpr int L_T    = 0;
constexpr int L_Q    = 131072;
constexpr int L_P    = L_Q + 16384;
constexpr int L_MX   = L_P + 8192;
constexpr int L_UKT  = L_Q;
constexpr int L_S1P  = L_Q + 4096;
constexpr int L_QB   = L_Q;
constexpr int L_QO   = L_Q + 2048;
constexpr int L_LAT  = L_Q;
constexpr int L_RS   = L_LAT + 2304;
constexpr int L_EX   = L_Q + 16384;
constexpr int L_ENDB = 163840;
constexpr int L_AQ   = 4096;
constexpr int L_AST  = L_AQ + 2048;
constexpr int L_AX   = L_AST + 20736;
constexpr int L_END  = L_ENDB;

DI u32 fsw(u32 key) { return (key & 2) | ((key >> 1) & 4); }
DI int ppos(int r, int c16) { return 32 * r + 2 * c16; }
DI int pcol(int p) { return 8 * ((p >> 1) & 15) + 2 * (p >> 5) + (p & 1); }
struct KArgs {
  const u16* xa; const u16* kc; const u16* vc; const u16* wqa; const u16* woa;
  const u16* xb; const u16* ckv; const u16* wqb; const u16* wuk; const u16* wuv; const u16* wob;
  char* ws; u16* out;
};

DI u32x4 ldnt(const u16* p) { return __builtin_nontemporal_load(reinterpret_cast<const u32x4*>(p)); }
DI u32x4 ldsh(const u16* p) { return *reinterpret_cast<const u32x4*>(p); }
DI u32x4 ld_sc1(__amdgpu_buffer_rsrc_t rs, u32 off) { return __builtin_amdgcn_raw_buffer_load_b128(rs, off, 0, 16); }
DI u32x2 ld_sc1_64(__amdgpu_buffer_rsrc_t rs, u32 off) { return __builtin_amdgcn_raw_buffer_load_b64(rs, off, 0, 16); }
DI void st_sc1(__amdgpu_buffer_rsrc_t rs, u32 off, u32x4 v) { __builtin_amdgcn_raw_buffer_store_b128(v, rs, off, 0, 16); }
DI void st_sc1_32(__amdgpu_buffer_rsrc_t rs, u32 off, u32 v) { __builtin_amdgcn_raw_buffer_store_b32(v, rs, off, 0, 16); }
DI void wg_barrier() { asm volatile("s_waitcnt lgkmcnt(0)\n\ts_barrier" ::: "memory"); }

DI u64 fx_add(char* ws, __amdgpu_buffer_rsrc_t rs, float y, u32 aoff, u32 xown, u32 tag) {
  const float t = __builtin_fabsf(y);
  u64 x = __builtin_bit_cast(u64, __builtin_fma((double)y, 0x1p40, 0x1.8p52)) + (0x0400000000000000ull - 0x4338000000000000ull);
  PIN(x);
  const u64 big = __ballot(!(t < 64.f)), some = __ballot(t >= 0x1p-24f);
  u64 odd;
  asm("s_cmp_eq_u64 %2, 0\n\ts_cselect_b64 %0, -1, %1" : "=s"(odd) : "s"(big), "s"(some) : "scc");
  if (__builtin_expect(odd != 0ull, 0)) {
    if (__ballot(t != 0.f) != 0ull) {
      st_sc1(rs, xown, u32x4{__float_as_uint(y), tag, ~__float_as_uint(y), tag});
      x = 0x0410000000000000ull;
      asm volatile("s_waitcnt vmcnt(0)" ::: "memory");
    }
  }
  return __hip_atomic_fetch_add(reinterpret_cast<u64*>(ws + aoff), x, __ATOMIC_RELAXED, __HIP_MEMORY_SCOPE_AGENT) + x;
}
template <int NARR, int XSTR>
DI float fx_val(__amdgpu_buffer_rsrc_t rs, u64 t, u32 x0, u32 tag) {
  float s = (float)__builtin_fma(__builtin_bit_cast(double, (t & 0xfffffffffffffull) | 0x4330000000000000ull), 0x1p-40, -0x1.8p12);
  const u32 np = (u32)(t >> 52) & 63u;
  if (__builtin_expect(np != 0u, 0)) {
    float e = 0.f;
#pragma unroll 1
    for (u32 spins = 0; spins < 64; ++spins) {
      u32 c = 0; e = 0.f;
#pragma unroll 4
      for (u32 p = 0; p < NARR; ++p) {
        const u32x4 v = ld_sc1(rs, x0 + p * XSTR);
        const bool ok = (v[1] == tag) & (v[3] == tag) & (v[0] == ~v[2]);
        e += ok ? __uint_as_float(v[0]) : 0.f; c += ok;
      }
      if (c == np) break;
    }
    s += e;
  }
  return s;
}
template <int CTRL, int BANK> DI u32 dppb(u32 old, u32 v) { return (u32)__builtin_amdgcn_update_dpp((int)old, (int)v, CTRL, 0xf, BANK, false); }
constexpr u32 OOB = 0x80000000u;
template <int N>
DI void row_issue(__amdgpu_buffer_rsrc_t rs, const u32 (&off)[N], u32x4 (&v)[N]) {
#pragma unroll
  for (int p = 0; p < N; ++p) v[p] = ld_sc1(rs, off[p]);
}
template <int N>
DI void row_wait(__amdgpu_buffer_rsrc_t rs, const u32 (&off)[N], u32x4 (&v)[N], u32 tag, bool act, int lane) {
  bool ok = true;
#pragma unroll
  for (int p = 0; p < N; ++p) PIN(v[p]);
#pragma unroll
  for (int p = 0; p < N; ++p) ok &= (v[p][3] == tag);
  if (__builtin_expect(!__all(ok | !act), 0)) {
#pragma unroll 1
    for (u32 spins = 0; spins < SPIN_CAP; ++spins) {
      __builtin_amdgcn_s_sleep(2);
      asm volatile("" ::: "memory");
      u32x4 t[N]; bool re[N];
#pragma unroll
      for (int p = 0; p < N; ++p) {
        const u64 m = __ballot(act & !((v[p][3] == tag)));
        re[p] = ((u32)(m >> (lane & 48)) & 0xffffu) != 0u;
        t[p] = ld_sc1(rs, re[p] ? off[p] : OOB);
      }
      bool k2 = true;
#pragma unroll
      for (int p = 0; p < N; ++p) { v[p] = re[p] ? t[p] : v[p]; PIN(v[p]); k2 &= (v[p][3] == tag); }
      if (__all(k2 | !act)) break;
    }
  }
}
template <int NS> DI void pace(u32 t0) {
  SCHED_FENCE();
  if (NS > 0)
    asm volatile("s_branch .Lpc%=\n.Lpl%=:\n\ts_sleep 1\n.Lpc%=:\n\ts_memrealtime s[98:99]\n\ts_waitcnt lgkmcnt(0)\n\ts_sub_u32 s98, s98, %0\n\ts_cmpk_lt_u32 s98, %1\n\ts_cbranch_scc1 .Lpl%="
                 :: "s"(t0), "n"(NS / 10) : "s98", "s99", "scc", "memory");
  SCHED_FENCE();
}
template <class F, int... I> DI void unroll_i(F f, std::integer_sequence<int, I...>) { (f(std::integral_constant<int, I>{}), ...); }
template <int N, class F> DI void unroll_c(F f) { unroll_i(f, std::make_integer_sequence<int, N>{}); }
#define IC(x) std::integral_constant<int, x>{}
#define INL __attribute__((always_inline))
DI int lane_id() { int l; asm volatile("v_mbcnt_lo_u32_b32 %0, -1, 0\n\tv_mbcnt_hi_u32_b32 %0, -1, %0" : "=v"(l)); return l; }
DI u32 clk32() { return (u32)__builtin_amdgcn_s_memrealtime(); }
DI void hint_wait(__amdgpu_buffer_rsrc_t rs, u32 off, u32x2 hv, u32 tag, int dt) {
  PIN(hv);
  if (__builtin_expect(!__all(hv[1] == tag), 0)) {
#pragma unroll 1
    for (u32 spins = 0; spins < 8; ++spins) {
      asm volatile("" ::: "memory");
      const u32x2 t = ld_sc1_64(rs, hv[1] != tag ? off : OOB);
      hv = hv[1] != tag ? t : hv;
      PIN(hv);
      if (__all(hv[1] == tag)) break;
    }
  }
  const u32 t0 = clk32();
  const float d = row16_maxq(hv[1] == tag ? (float)(int)(hv[0] - t0) : -1000.f);
  const int n = min(__builtin_amdgcn_readfirstlane((int)d) + dt, 250);
#pragma unroll 1
  for (int i = 0; i < 256 && (int)(clk32() - t0) < n; ++i) __builtin_amdgcn_s_sleep(1);
  asm volatile("" ::: "memory");
}
DI void both_halves(__amdgpu_buffer_rsrc_t rs, u32 off, u32x4& hv, u32 tag) {
  PIN(hv);
  u32 m = (u32)__ballot((hv[0] == tag) & (hv[1] == tag) & (hv[3] == tag));
  if (__builtin_expect(!(m & 0xffu) | !(m & 0xff00u), 0)) {
#pragma unroll 1
    for (u32 spins = 0; spins < SPIN_CAP; ++spins) {
      __builtin_amdgcn_s_sleep(2);
      asm volatile("" ::: "memory");
      hv = ld_sc1(rs, off);
      PIN(hv);
      m = (u32)__ballot((hv[0] == tag) & (hv[1] == tag) & (hv[3] == tag));
      if ((m & 0xffu) && (m & 0xff00u)) break;
    }
  }
}
DI u32 next_tag(u32 e) { return e * 1664525u + 1013904223u; }

DI void s1_mfma(const u32x4 (&R)[2][8], const u32x4 (&xf)[2], int r, f32x4& s0, f32x4& s1) {
  f32x4 acc[8];
#pragma unroll
  for (int j = 0; j < 8; ++j) acc[j] = mfma16(asbf(xf[0]), bfrag(R[0], j), f32x4{0.f, 0.f, 0.f, 0.f});
#pragma unroll
  for (int j = 0; j < 8; ++j) PIN(acc[j]);
  SCHED_FENCE();
#pragma unroll
  for (int j = 0; j < 8; ++j) acc[j] = mfma16(asbf(xf[1]), bfrag(R[1], j), acc[j]);
  s0 = sel4(acc[0], acc[2], acc[4], acc[6], r); s1 = sel4(acc[1], acc[3], acc[5], acc[7], r);
}
DI int qoffs(int qd, int rot) { const int aq = (qd + rot) & 3; return 32 * (aq >> 1) + 4 * (aq & 1); }
DI void x_load(const u16* xsrc, int rot, u32x4 (&xf)[2]) {
#pragma unroll
  for (int k = 0; k < 2; ++k) {
    const u32x2 lo = *reinterpret_cast<const u32x2*>(xsrc + qoffs(2 * k, rot)), hi = *reinterpret_cast<const u32x2*>(xsrc + qoffs(2 * k + 1, rot));
    xf[k] = u32x4{lo[0], lo[1], hi[0], hi[1]};
  }
}
constexpr int trow_t(int i) { return i < XR ? BT0 + i * BD1 : i < X0 ? BT0 + (i + 4) * BD1 : BT0 + (X0 + 4) * BD1 + 12 * BDQ + (i - X0) * BD2; }
constexpr int wq_t(int n) { return n < 4 ? BT0 + (XR + n) * BD1 : BT0 + (X0 + 4) * BD1 + (n - 4) * BDQ; }
template <int LO, int HI> DI void s1_load(const u16* wsrc, int rot, u32x4 (&R)[2][8], u32 t0) {
  unroll_c<4 * (HI - LO)>([&](auto k) {
    constexpr int n = 4 * LO + k, qd = n >> 2, i = n & 3;
    if (i == 0 && qd) { SCHED_FENCE(); __builtin_amdgcn_s_barrier(); }
    pace<wq_t(n)>(t0);
    R[qd >> 1][4 * (qd & 1) + i] = ldsh(wsrc + (size_t)(qoffs(qd, rot) + i) * (NH * DH));
  });
}

DI float score4(const u32x4 (&k)[4], const u32x4& qpk, bool hi8, bool hi4) {
  const float d0 = dot8(k[0], qpk), d1 = dot8(k[1], qpk), d2 = dot8(k[2], qpk), d3 = dot8(k[3], qpk);
  const float a0 = (hi8 ? d2 : d0) + dppf<0x128>(hi8 ? d0 : d2), a1 = (hi8 ? d3 : d1) + dppf<0x128>(hi8 ? d1 : d3);
  float s = (hi4 ? a1 : a0) + dppf<0x141>(hi4 ? a0 : a1);
  s += dppf<0xB1>(s); s += dppf<0x4E>(s);
  return s * SCL2;
}
DI s16x4 ppack(float p) { return __builtin_bit_cast(s16x4, u32x2{pk2(p, dppf<0x12c>(p)), pk2(dppf<0x128>(p), dppf<0x124>(p))}); }
DI void q_finish(__amdgpu_buffer_rsrc_t rs, u32 off, u32x4 v, u32 tag, char* dst, int lane) {
  PIN(v);
  if (__builtin_expect(!__all((v[1] == tag) & (v[3] == tag)), 0)) {
#pragma unroll 1
    for (u32 spins = 0; spins < SPIN_CAP; ++spins) {
      asm volatile("" ::: "memory");
      v = ld_sc1(rs, off);
      PIN(v);
      if (__all((v[1] == tag) & (v[3] == tag))) break;
      __builtin_amdgcn_s_sleep(1);
    }
  }
  float q0 = __uint_as_float(v[0]), q1 = __uint_as_float(v[2]);
  q0 += dppf<0x128>(q0); q1 += dppf<0x128>(q1);
  q0 = x16_sum(q0); q1 = x16_sum(q1);
  if (lane < 8) *reinterpret_cast<u32*>(dst) = pk2(q0, q1);
}

DI void roleB(const KArgs& a, char* smem, const int g, const int idx, const int w, const int lane0, const u32x4 (&uk)[4], const u32 tvo, const u32 cpf, const u32 t0) {
  const int h = 4 * (idx & 7) + ((idx >> 3) & 3), e4 = idx >> 5;
  const int b3 = idx & 3, kp = 2 * (idx >> 4) + ((idx >> 2) & 1), hh = (idx >> 3) & 1;
  const __amdgpu_buffer_rsrc_t rs = __builtin_amdgcn_make_buffer_rsrc((void*)a.ws, 0, (int)WS_END, RSRC_W3);
  const __amdgpu_buffer_rsrc_t rt = __builtin_amdgcn_make_buffer_rsrc((void*)a.ckv, 0, NB * SK * DC * 2, RSRC_W3);
  u32 tag = *reinterpret_cast<const u32*>(a.ws + WS_EPOCH);
  u32x4 tv[32], uv[4], ow[4];
  auto wload = [&](int pos) INL {
    if (pos == UVP) {
      const int l = lane_id();
      const char* p = reinterpret_cast<const char*>(a.wuv + ((size_t)(128 * e4 + 16 * w) * NH + h) * DH);
      const u32 o = (l >> 4) * (8 * NH * DH) + (l & 15) * 16;
#pragma unroll
      for (int n = 0; n < 4; ++n) uv[n] = ldnt(reinterpret_cast<const u16*>(p + n * (NH * DH * 2) + o));
    }
    if (pos == OWP) {
      const int l = lane_id();
      const char* p = reinterpret_cast<const char*>(a.wob + (size_t)(h * DH + 16 * w) * DM + 128 * e4);
      const u32 o = (l >> 4) * (8 * DM) + (l & 15) * 16;
#pragma unroll
      for (int n = 0; n < 4; ++n) ow[n] = ldnt(reinterpret_cast<const u16*>(p + n * (DM * 2) + o));
    }
  };
  const u32 tvl = tvo & ~(8u << 17), tso = (u32)hh << 20, tro = (idx >> 4) + (idx & 3);
  auto cluster = [&](auto i) INL {
    unroll_c<sb(i) - sb(i - 1)>([&](auto k0) INL { constexpr int k = sb(i - 1) + k0; tv[2 * (k & 8) + 8 + (k & 7)] = __builtin_amdgcn_raw_buffer_load_b128(rt, tvl, (tso ^ (1u << 20)) + ((k + tro) & 15) * 8192, 0); });
  };
  auto tload = [&](auto lo, auto hi) INL {
    unroll_c<hi - lo>([&](auto i0) INL {
      constexpr int i = lo + i0;
      pace<trow_t(i)>(t0);
      if constexpr (i != X0) cluster(IC(i));
      tv[2 * (i & 8) + (i & 7)] = __builtin_amdgcn_raw_buffer_load_b128(rt, tvl, tso + ((i + tro) & 15) * 8192, 0);
      SCHED_FENCE();
    });
  };
  if (w >= 6) st_sc1(rs, WS_ACC + (u32)((idx * 2 + w - 6) * 64 + lane0) * 16, u32x4{0u, 0x80000u, 0u, 0x80000u});
  SCHED_FENCE();
  constexpr int QN = 3;
  u32x4 qv[QN]; u32 qoff, pa64[QN], pa32[QN], ack, atf[2], atw;
  {
    const int lane = lane0, r = lane >> 4, c16 = lane & 15;
#pragma unroll
    for (int k = 0; k < QN; ++k) {
      const int G = 64 * k + lane, hl = 8 * (w & 1) + G / 24, rec = G % 24, u = rec / 3, j = rec - 3 * u, sc = 16 * (w >> 1) + 2 * u;
      pa64[k] = L_Q + hl * 1024 + (((sc + (j >> 1)) ^ hl) << 4) + 8 * (j & 1);
      pa32[k] = L_Q + hl * 1024 + (((sc + 1) ^ hl) << 4) + (j == 2 ? 4 : 8 + 4 * j);
      PIN(pa64[k]); PIN(pa32[k]);
    }
    ack = L_T + (16 * w + c16) * 128 + ((r ^ fsw(c16)) << 4);
    const u32 tq = c16 >> 2, tp = lane & 3;
#pragma unroll
    for (int u = 0; u < 2; ++u) { const u32 rr = 8 * r + tq + 4 * u; atf[u] = L_T + w * 16384 + rr * 128 + (((tp >> 1) ^ fsw(rr)) << 4) + 8 * (tp & 1); PIN(atf[u]); }
    atw = L_T + (lane >> 3) * 16384 + w * 128 + (((lane & 7) ^ fsw(w)) << 4);
    PIN(ack); PIN(atw);
  }
  auto twrite = [&](int half, int lo, int hi) INL {
    SCHED_FENCE();
    u32 aw[2] = {atw, atw};
    PIN(aw[1]);
    aw[1] ^= 64u;
#pragma unroll
    for (int i = 0; i < 17; ++i) {
      if (i < lo || i >= hi) continue;
#pragma unroll
      for (int k = 0; k < 16; ++k)
        if (k >= sb(i - 1) && k < sb(i) && (k >> 3) == half) *reinterpret_cast<LDSP u32x4*>((LDSP char*)aw[k & 1] + (8 + (k & 7)) * 1024) = tv[2 * (k & 8) + 8 + (k & 7)];
      if (i < 16 && (i >> 3) == half) *reinterpret_cast<LDSP u32x4*>((LDSP char*)aw[i & 1] + (i & 7) * 1024) = tv[2 * (i & 8) + (i & 7)];
    }
    SCHED_FENCE();
  };
  SCHED_FENCE();
  {
    const int lane = lane0, tid = w * 64 + lane, r = lane >> 4, c16 = lane & 15;
    u32x4 ua[4];
    const int m0 = 64 * w + 8 * r;
    const u16* wsrc = a.wqb + (size_t)m0 * (NH * DH) + h * DH + 8 * c16;
    u32x4 R[2][8], xf[2];
    x_load(a.xb + (c16 & 3) * DM + m0, e4, xf);
    unroll_c<X0 + 1>([&](auto i) INL {
      if constexpr (i == UKR) {
        SCHED_FENCE();
        asm volatile("" :: "v"(cpf));
        tag = next_tag(__builtin_amdgcn_readfirstlane(tag));
        char* us = smem + L_UKT + w * 4096;
#pragma unroll
        for (int k = 0; k < 4; ++k) *reinterpret_cast<u32x4*>(us + (4 * r + k) * 256 + ((c16 ^ (4 * r + k)) << 4)) = uk[k];
        asm volatile("" ::: "memory");
#pragma unroll
        for (int k = 0; k < 4; ++k) ua[k] = *reinterpret_cast<const u32x4*>(us + c16 * 256 + (((4 * k + r) ^ c16) << 4));
#pragma unroll
        for (int k = 0; k < 4; ++k) PIN(ua[k]);
      }
      if constexpr (i == XR) s1_load<0, 1>(wsrc, e4, R, t0);
      if constexpr (i < X0) tload(i, IC(i + 1));
      if constexpr (i < X0 && i >= WLG) twrite(0, i - WLG, i - WLG + 1);
    });
    pace<wq_t(4)>(t0);
    cluster(IC(X0));
    s1_load<1, 4>(wsrc, e4, R, t0);
    SCHED_FENCE();
    tload(IC(X0), IC(X0 + TI));
    twrite(0, X0 - WLG, X0 + 1);
    f32x4 s0, s1;
    s1_mfma(R, xf, r, s0, s1);
    float* pw = reinterpret_cast<float*>(smem + L_S1P) + w * 512 + ppos(r, c16);
#pragma unroll
    for (int b = 0; b < NB; ++b) *reinterpret_cast<u32x2*>(pw + b * DH) = u32x2{__float_as_uint(s0[b]), __float_as_uint(s1[b])};
    SCHED_FENCE();
    tload(IC(X0 + TI), IC(X0 + TI + T2N));
    wg_barrier();
    SCHED_FENCE();
    tload(IC(X0 + TI + T2N), IC(TH));
    const float* pp = reinterpret_cast<const float*>(smem + L_S1P) + tid;
    const float s = ((pp[0] + pp[512]) + (pp[1024] + pp[1536])) + ((pp[2048] + pp[2560]) + (pp[3072] + pp[3584]));
    *reinterpret_cast<u16*>(smem + L_QB + (tid >> 7) * 288 + pcol(tid & 127) * 2) = (u16)pk2(s, s);
    wg_barrier();
    SCHED_FENCE();
    tload(IC(TH), IC(TH + T4N));
    f32x4 d0 = f32x4{0.f, 0.f, 0.f, 0.f};
#pragma unroll
    for (int i = 0; i < 4; ++i) {
      const bf16x8 qf = *reinterpret_cast<const bf16x8*>(smem + L_QB + (c16 & 3) * 288 + (32 * i + 8 * r) * 2);
      d0 = mfma16(asbf(ua[i]), qf, d0);
    }
    if (c16 < 4) {
      char* o = smem + L_QO + w * 256 + c16 * 48;
      *reinterpret_cast<u32*>(o + (r < 3 ? 16 * r : 8)) = pk2(d0[0], d0[1]);
      *reinterpret_cast<u32*>(o + (r < 3 ? 16 * r + 4 : 24)) = pk2(d0[2], d0[3]);
    }
    wg_barrier();
    if (w < 4 && lane < 24) {
      u32x4 x = *reinterpret_cast<const u32x4*>(smem + L_QO + (lane / 3) * 256 + (3 * w + lane % 3) * 16);
      x[3] = tag; x[2] = lane % 3 == 2 ? x[1] : x[2];
      st_sc1(rs, WS_QP + (u32)(((w * 4 + e4) * 32 + h) * 24 + lane) * 16, x);
    }
  }
  SCHED_FENCE();
  u32x4 ck[2][16];
  u32 ac[4], at[4][2];
  auto ckaddr = [&]() INL { PIN(ack); ac[0] = ack; ac[1] = ack ^ 64u; ac[2] = ac[0] + 65536u; ac[3] = ac[1] + 65536u; };
  auto tfaddr = [&]() INL {
#pragma unroll
    for (int u = 0; u < 2; ++u) {
      PIN(atf[u]);
#pragma unroll
      for (int ct = 0; ct < 4; ++ct) at[ct][u] = atf[u] ^ (u32)(ct << 5);
    }
  };
  auto ckread = [&](int kb, int s) INL { ck[kb][s] = *reinterpret_cast<const LDSP u32x4*>((const LDSP char*)ac[2 * (s >> 3) + (s & 1)] + ((s >> 1) & 3) * 16384); };
  bf16x8 tf[2][8][2];
  auto tfread = [&](int kb, int k2) INL {
#pragma unroll
    for (int cb = 0; cb < 2; ++cb) {
      s16x4 b0 = __builtin_amdgcn_ds_read_tr16_b64_v4i16((LDSP s16x4*)((LDSP char*)at[2 * (k2 & 1) + cb][0] + 4096 * (k2 >> 1)));
      s16x4 b1 = __builtin_amdgcn_ds_read_tr16_b64_v4i16((LDSP s16x4*)((LDSP char*)at[2 * (k2 & 1) + cb][1] + 4096 * (k2 >> 1)));
      tf[kb][k2][cb] = __builtin_shufflevector(b0, b1, 0, 1, 2, 3, 4, 5, 6, 7);
    }
  };
  constexpr int R0 = TH + T4N, R1 = (R0 + 17) / 2;
  tload(IC(R0), IC(R1));
  tfaddr();
#pragma unroll
  for (int k2 = 0; k2 < 8; ++k2) tfread(0, k2);
  SCHED_FENCE();
  tload(IC(R1), IC(16));
  ckaddr();
#pragma unroll
  for (int s = 0; s < 16; ++s) ckread(0, s);
  SCHED_FENCE();
  wg_barrier();
#pragma unroll
  for (int s = 0; s < 16; ++s) { PIN(ck[0][s]); PIN(tf[0][s >> 1][s & 1]); }
  SCHED_FENCE();
  cluster(IC(16));
  twrite(1, 0, 17);
  qoff = WS_QP + (u32)((b3 * 8 + (w & 6) + hh) * 384 + 192 * (w & 1) + lane_id()) * 16;
#pragma unroll
  for (int k = 0; k < QN; ++k) qv[k] = ld_sc1(rs, qoff + 1024 * k);
  SCHED_FENCE();
  wg_barrier();
  auto so = [](int n) { return (n >> 2) + 4 * (n & 3); };
  ckaddr();
#pragma unroll
  for (int n = 0; n < CK1; ++n) ckread(1, so(n));
  u32 aq, amw, hco;
  {
    const int lane = lane_id(), r = lane >> 4, c16 = lane & 15;
    aq = L_Q + c16 * 1024 + ((r ^ c16) << 4);
    amw = L_MX + (w >> 2) * 256 + c16 * 16 + (w & 3) * 4;
    PIN(aq); PIN(amw);
    SCHED_FENCE();
    const u32 qo[QN] = {qoff, qoff + 1024, qoff + 2048};
    row_wait<QN>(rs, qo, qv, tag, true, lane);
    SCHED_FENCE();
#pragma unroll
    for (int k = 0; k < QN; ++k) {
      *reinterpret_cast<LDSP u32x2*>((LDSP char*)pa64[k]) = u32x2{qv[k][0], qv[k][1]};
      *reinterpret_cast<LDSP u32*>((LDSP char*)pa32[k]) = qv[k][2];
    }
    SCHED_FENCE();
    wload(3);
    SCHED_FENCE();
    wg_barrier();
    SCHED_FENCE();
  }
  u32x4 hc;
  SCHED_FENCE();
  {
    const u32 tsc = clk32();
    f32x4 sc[2], sd[2];
    u32x4 qa[16];
    u32 amx, apw, amr, apf[4], gb, gm, lane, r, c16;
    auto qread = [&](int n) INL { qa[n] = *reinterpret_cast<const LDSP u32x4*>((const LDSP char*)(aq ^ (u32)(64 * (n >> 2))) + (n & 3) * 256); };
#pragma unroll
    for (int n = 0; n < QA; ++n) qread(n);
#pragma unroll
    for (int n = 0; n < 16; ++n) {
      SCHED_FENCE();
      if (n + QA < 16) qread(n + QA);
      if (n + CK1 < 16) ckread(1, so(n + CK1));
      SCHED_FENCE();
#pragma unroll
      for (int kb = 0; kb < 2; ++kb) {
        if (n & 1) sd[kb] = mfma16(asbf(ck[kb][so(n)]), asbf(qa[n]), n > 1 ? sd[kb] : f32x4{0.f, 0.f, 0.f, 0.f});
        else sc[kb] = mfma16(asbf(ck[kb][so(n)]), asbf(qa[n]), n ? sc[kb] : f32x4{0.f, 0.f, 0.f, 0.f});
      }
      if (n == 0) st_sc1(rs, w == 7 && lane_id() == 0 ? WS_FLG + (u32)idx * 128 : OOB, u32x4{tag, tag, tsc, tag});
      if (n == 8) {
        lane = lane_id(); r = lane >> 4; c16 = lane & 15;
        amx = L_MX + c16 * 16;
        apw = L_P + c16 * 512 + (((2 * w + (r >> 1)) ^ c16) << 4) + 8 * (r & 1);
        PIN(amx); PIN(apw);
      }
      if (n == 9) { amr = L_MX + (c16 & 4) * 128 + (4 * r + (c16 & 2) + (c16 >> 3)) * 16; PIN(amr); }
      if (n == 10) {
        gb = WS_PART + (u32)((b3 * 16 + kp) * 8 + w) * PBLK; gm = gb + 4096 + hh * 256 + lane * 4;
        gb += hh * 2048 + r * 512 + c16 * 16;
        hco = WS_FLG + (u32)((w & 3) + 4 * (c16 & 1) + (c16 & 8) + 16 * ((w & 4) + ((c16 & 7) >> 1))) * 128;
        PIN(gb); PIN(gm); PIN(hco);
      }
      if (n == 11) {
#pragma unroll
        for (int k = 0; k < 4; ++k) { apf[k] = (L_P + c16 * 512 + ((r ^ c16) << 4)) ^ (u32)(k << 6); PIN(apf[k]); }
      }
      if (n == 12) {
        tfaddr();
#pragma unroll
        for (int k = 0; k < 8; ++k) PIN(at[k >> 1][k & 1]);
      }
    }
    SCHED_FENCE();
    {
      sc[0] += sd[0]; sc[1] += sd[1];
      *reinterpret_cast<LDSP float*>((LDSP char*)amw) = x64_maxq(max8q(sc[0], sc[1])) * SCL2;
    }
    SCHED_FENCE();
    wload(4);
    SCHED_FENCE();
    wg_barrier();
    SCHED_FENCE();
    {
      const f32x4 a0 = *reinterpret_cast<const LDSP f32x4*>((const LDSP char*)amx), a1 = *reinterpret_cast<const LDSP f32x4*>((const LDSP char*)amx + 256);
      SCHED_FENCE();
#pragma unroll
      for (int k2 = 0; k2 < TFS; ++k2) tfread(1, k2);
      SCHED_FENCE();
      const float m = max8q(a0, a1);
      const f32x4 x0 = sc[0], x1 = sc[1];
      u32x2 p0, p1;
      p0[0] = pk2(__builtin_amdgcn_exp2f(x0[0] * SCL2 - m), __builtin_amdgcn_exp2f(x0[1] * SCL2 - m));
      p0[1] = pk2(__builtin_amdgcn_exp2f(x0[2] * SCL2 - m), __builtin_amdgcn_exp2f(x0[3] * SCL2 - m));
      p1[0] = pk2(__builtin_amdgcn_exp2f(x1[0] * SCL2 - m), __builtin_amdgcn_exp2f(x1[1] * SCL2 - m));
      p1[1] = pk2(__builtin_amdgcn_exp2f(x1[2] * SCL2 - m), __builtin_amdgcn_exp2f(x1[3] * SCL2 - m));
      *reinterpret_cast<LDSP u32x2*>((LDSP char*)apw) = p0;
      *reinterpret_cast<LDSP u32x2*>((LDSP char*)apw + 256) = p1;
      SCHED_FENCE();
      const float ls = x32_sum(x16_sum(dot2(p0[1], 0x3f803f80u, dot2(p0[0], 0x3f803f80u, 0.f)) + dot2(p1[1], 0x3f803f80u, dot2(p1[0], 0x3f803f80u, 0.f))));
      *reinterpret_cast<LDSP float*>((LDSP char*)amw + 512) = ls;
    }
    wg_barrier();
    SCHED_FENCE();
    hc = ld_sc1(rs, hco);
    SCHED_FENCE();
    u32x4 pf[8];
    f32x4 m0, m1;
    auto pread = [&](int k) INL { pf[k] = *reinterpret_cast<const LDSP u32x4*>((const LDSP char*)apf[k & 3] + (k >> 2) * 256); };
#pragma unroll
    for (int k = 0; k < 4; ++k) { pread(k); SCHED_FENCE(); }
    wload(5);
    SCHED_FENCE();
    f32x4 acc[4];
    u32 cs[2], mw;
    auto pack = [&](int ip) INL {
      const u32 a0 = pk2(acc[0][2 * ip], acc[1][2 * ip]), a1 = pk2(acc[2][2 * ip], acc[3][2 * ip]);
      const u32 b0 = pk2(acc[0][2 * ip + 1], acc[1][2 * ip + 1]), b1 = pk2(acc[2][2 * ip + 1], acc[3][2 * ip + 1]);
      const u32x4 x = u32x4{dppb<0x128, 0xc>(a0, b0) + tag, dppb<0x128, 0xc>(a1, b1), dppb<0x128, 0x3>(b0, a0), dppb<0x128, 0x3>(b1, a1)};
      st_sc1(rs, gb + ip * 256, x);
      u32 c = (x[0] + x[1]) + (x[2] + x[3]);
      c += dppu<0xB1>(c); c += dppu<0x4E>(c);
      cs[ip] = c;
      PIN(cs[ip]);
    };
#pragma unroll
    for (int n = 0; n < 32; ++n) {
      acc[n & 3] = mfma16(asbf(pf[n >> 2]), tf[n >> 4][2 * ((n >> 2) & 3) + ((n >> 1) & 1)][n & 1], n < 4 ? f32x4{0.f, 0.f, 0.f, 0.f} : acc[n & 3]);
      SCHED_FENCE();
      if (n % 3 == 0 && n / 3 < 4) pread(4 + n / 3);
      if (n % 2 == 1 && TFS + n / 2 < 8) tfread(1, TFS + n / 2);
      if (n == 18) { m0 = *reinterpret_cast<const LDSP f32x4*>((const LDSP char*)amr); m1 = *reinterpret_cast<const LDSP f32x4*>((const LDSP char*)amr + 256); }
      if (n == 27) {
        PIN(m0); PIN(m1);
        const float mx = max8q(m0, m1);
        const float sm = ((m0[0] + m0[1]) + (m0[2] + m0[3])) + ((m1[0] + m1[1]) + (m1[2] + m1[3]));
        mw = __float_as_uint((c16 & 4) ? sm : mx);
        PIN(mw);
      }
      SCHED_FENCE();
    }
    SCHED_FENCE();
    pack(0); pack(1);
    st_sc1_32(rs, gm, mw + ((c16 & 1) ? ((c16 & 2) ? cs[1] : cs[0]) + tag : 0u));
    SCHED_FENCE();
  }
  const int lane = lane_id(), tid = w * 64 + lane, r = lane >> 4, c16 = lane & 15;
  s16x4 owf[8]; bf16x8 uvf[4];
  auto uvwrite = [&]() {
#pragma unroll
    for (int n = 0; n < 4; ++n) {
      const int c = 16 * w + 4 * r + n;
      *reinterpret_cast<u32x4*>(smem + L_T + w * 16384 + (4 * r + n) * 256 + ((c16 ^ (2 * (((c >> 4) & 3) | ((c & 1) << 2)))) << 4)) = uv[n];
    }
  };
  auto uvread = [&]() {
    const u32 tq = c16 >> 2, tp = lane & 3;
#pragma unroll
    for (int s = 0; s < 4; ++s) {
      const u32 c0 = 64 * (s >> 1) + 16 * tq + 4 * (s & 1) + r, sw = 2 * (tq | (4 * (r & 1)));
      const char* p = smem + L_T + (c0 >> 4) * 16384 + (c0 & 15) * 256 + (((2 * w + (tp >> 1)) ^ sw) << 4) + 8 * (tp & 1);
      s16x4 b0 = __builtin_amdgcn_ds_read_tr16_b64_v4i16((LDSP s16x4*)p);
      s16x4 b1 = __builtin_amdgcn_ds_read_tr16_b64_v4i16((LDSP s16x4*)(p + 8 * 256));
      uvf[s] = __builtin_shufflevector(b0, b1, 0, 1, 2, 3, 4, 5, 6, 7);
      if (UVP < 6) PIN(uvf[s]);
    }
  };
  auto owpack = [&]() {
#pragma unroll
    for (int j = 0; j < 8; ++j) { owf[j] = kfrag(ow, j); PIN(owf[j]); }
  };
  if (UVP < 6) uvwrite();
  wg_barrier();
  SCHED_FENCE();
  wload(6);
  SCHED_FENCE();
  const u32 pblk = WS_PART + (u32)(((w & 3) * 16 + 8 * (w >> 2) + r) * 8 + 2 * e4 + (c16 >> 3)) * PBLK;
  u32 poff = pblk + h * 128 + ((c16 & 7) << 4), moff = pblk + (c16 & 1) * (32 * PBLK) + 4096 + (4 * (h >> 2) + 2 * (h & 1) + ((c16 >> 2) & 1)) * 16 + 4 * (h & 2);
  PIN(moff);
  u32 lrd = L_LAT + (c16 & 3) * 288 + 16 * r, mrd = L_RS + 16 * (c16 & 3);
  const u32 word = (tid >> 7) * 512 + 128 * e4 + (tid & 127);
  u32 cxa = WS_ACX + (u32)(h * 512 + tid) * 8, exw = L_EX + (w * 512 + ppos(r, c16)) * 4;
  u32 aoff = WS_ACC + 65536 + (word >> 3) * 256 + (word & 7) * 8, ooff = ((tid >> 7) * 1024 + 512 + 128 * e4 + pcol(tid & 127)) * 2;
  {
    u32 off[2]; u32x4 v[2]; u32x2 mv;
#pragma unroll
    for (int t = 0; t < 2; ++t) { off[t] = poff + (u32)(t * 32 * PBLK); PIN(off[t]); }
    both_halves(rs, hco, hc, tag);
    if (idx == 0 && w == 7) {
      const u32 eoff = WS_QX + (u32)((lane >> 2) * 16 + (lane & 3)) * DH * 8;
#pragma unroll 1
      for (u32 spins = 0; spins < SPIN_CAP; ++spins) {
        const u32x4 e0 = ld_sc1(rs, eoff), e1 = ld_sc1(rs, eoff + 16 * 16 * DH * 8);
        if (__all((e0[1] == tag) & (e0[3] == tag) & (e1[1] == tag) & (e1[3] == tag))) break;
        asm volatile("" ::: "memory");
      }
      if (lane == 0) st_sc1_32(rs, WS_EPOCH, tag);
    }
    hint_wait(rs, hco + 8, (c16 >> 3) == (h >> 4) ? u32x2{hc[2], hc[3]} : u32x2{clk32() - 1000u, tag}, tag, THC);
    asm volatile("" ::: "memory");
    mv = ld_sc1_64(rs, moff);
    SCHED_FENCE();
    row_issue<2>(rs, off, v);
    SCHED_FENCE();
    wload(7);
    SCHED_FENCE();
    if (UVP < 6) uvread();
    if (UVP == 6) uvwrite();
    if (OWP < 7) owpack();
    u32 lad = L_LAT + w * 288 + c16 * 16 + 4 * r, rsa = L_RS + 16 * (w & 3) + 8 * (w >> 2) + (lane & 4);
    PIN(rsa); PIN(lrd); PIN(mrd);
    PIN(lad); PIN(ooff); PIN(aoff); PIN(cxa); PIN(exw);
    SCHED_FENCE();
    auto bad = [&]() {
      u32 S[2];
#pragma unroll
      for (int t = 0; t < 2; ++t) S[t] = (v[t][0] + v[t][1]) + (v[t][2] + v[t][3]);
      const bool o1 = lane & 1;
      const u32 u0 = (o1 ? S[1] : S[0]) + dppu<0xB1>(o1 ? S[0] : S[1]);
      const u32 s = u0 + dppu<0x4E>(u0);
      return __ballot(tag + mv[0] + s != mv[1]);
    };
    s16x4 cf; float ml;
    auto weights = [&]() {
      float M = __uint_as_float(mv[0]);
      asm volatile("s_nop 1\n\tv_max_f32_dpp %0, %0, %0 quad_perm:[1,0,3,2] row_mask:0xf bank_mask:0xf\n\ts_nop 1" : "+v"(M));
      M = x32_max(x16_max(M));
      const float ex = __builtin_amdgcn_exp2f(__uint_as_float(mv[0]) - M);
      u32 eb = pk2(ex, ex);
      eb = dppb<0x114, 0xa>(eb, eb);
      cf = __builtin_bit_cast(s16x4, u32x2{plo(dppu<0x55>(eb), dppu<0x00>(eb)), 0u});
      float ls = bflo(eb) * __uint_as_float(mv[0]);
      ls += dppf<0xB1>(ls);
      ls = x32_sum(x16_sum(ls));
      ml = (lane & 4) ? ls : M;
      PIN(cf); PIN(ml);
    };
    weights();
    SCHED_FENCE();
    u64 bm = bad();
    if (__builtin_expect(bm != 0ull, 0)) {
#pragma unroll 1
      for (u32 spins = 0; spins < SPIN_CAP; ++spins) {
        __builtin_amdgcn_s_sleep(2);
        asm volatile("" ::: "memory");
        const u32 q = (u32)(bm >> (lane & 60));
        u32x4 t4[2];
#pragma unroll
        for (int t = 0; t < 2; ++t) t4[t] = ld_sc1(rs, ((q >> t) & 1) ? off[t] : OOB);
        const bool rm = (q >> (lane & 3)) & 1;
        const u32x2 t2 = ld_sc1_64(rs, rm ? moff : OOB);
#pragma unroll
        for (int t = 0; t < 2; ++t) v[t] = ((q >> t) & 1) ? t4[t] : v[t];
        mv = rm ? t2 : mv;
        weights();
        bm = bad();
        if (bm == 0ull) break;
      }
    }
    SCHED_FENCE();
#pragma unroll
    for (int t = 0; t < 2; ++t) v[t][0] -= tag;
    f32x4 d[8];
#pragma unroll
    for (int j = 0; j < 8; ++j) d[j] = __builtin_amdgcn_mfma_f32_16x16x16bf16_1k(cf, __builtin_bit_cast(s16x4, u32x2{(j & 1) ? phi(v[1][j >> 1], v[0][j >> 1]) : plo(v[1][j >> 1], v[0][j >> 1]), 0u}), f32x4{0.f, 0.f, 0.f, 0.f}, 0, 0, 0);
    if ((lane & ~4) == 0) *reinterpret_cast<LDSP float*>((LDSP char*)rsa) = ml;
    SCHED_FENCE();
    wload(8);
    SCHED_FENCE();
#pragma unroll
    for (int j = 0; j < 8; ++j) PIN(d[j]);
    const float x0 = (r & 2) ? ((r & 1) ? d[6][0] : d[4][0]) : ((r & 1) ? d[2][0] : d[0][0]), x1 = (r & 2) ? ((r & 1) ? d[7][0] : d[5][0]) : ((r & 1) ? d[3][0] : d[1][0]);
    *reinterpret_cast<LDSP u32*>((LDSP char*)lad) = pk2(x0, x1);
    if (UVP == 7) uvwrite();
  }
  wg_barrier();
  SCHED_FENCE();
  {
    if (UVP >= 6) uvread();
    f32x4 c0 = f32x4{0.f, 0.f, 0.f, 0.f}, c1 = c0;
    const f32x4 ml = *reinterpret_cast<const LDSP f32x4*>((const LDSP char*)mrd);
    SCHED_FENCE();
    bf16x8 lf[2][4];
#pragma unroll
    for (int s = 0; s < 4; ++s) { lf[0][s] = *reinterpret_cast<const LDSP bf16x8*>((const LDSP char*)lrd + 64 * s); lf[1][s] = *reinterpret_cast<const LDSP bf16x8*>((const LDSP char*)lrd + 1152 + 64 * s); }
    SCHED_FENCE();
#pragma unroll
    for (int s = 0; s < 4; ++s) { c0 = mfma16(uvf[s], lf[0][s], c0); c1 = mfma16(uvf[s], lf[1][s], c1); }
    const float M = fmaxf(ml[0], ml[2]), a0 = __builtin_amdgcn_exp2f(ml[0] - M), a1 = __builtin_amdgcn_exp2f(ml[2] - M);
    const float rsum = __builtin_amdgcn_rcpf(fmaf(a0, ml[1], a1 * ml[3]));
    const bool i1 = c16 & 4, i2 = c16 & 8;
    float y0 = i1 ? c0[1] : c0[0], y1 = i1 ? c0[3] : c0[2], z0 = i1 ? c1[1] : c1[0], z1 = i1 ? c1[3] : c1[2];
    PIN(y0); PIN(y1); PIN(z0); PIN(z1);
    const float mine = fmaf(a0 * rsum, i2 ? y1 : y0, (a1 * rsum) * (i2 ? z1 : z0));
    u64 t = fx_add(a.ws, rs, mine, cxa, WS_XC + (u32)((h * 4 + e4) * 512 + tid) * 16, tag);
    if (OWP >= 7) owpack();
#pragma unroll 1
    for (u32 spins = 0; spins < SPIN_CAP; ++spins) {
      if (__all((u32)(t >> 58) == 4u)) break;
      u64 z = 0; PIN(z);
      asm volatile("" ::: "memory");
      if (__builtin_expect(spins < 6, 1)) t = __hip_atomic_load(reinterpret_cast<u64*>(a.ws + cxa), __ATOMIC_RELAXED, __HIP_MEMORY_SCOPE_AGENT);
      else t = __hip_atomic_fetch_add(reinterpret_cast<u64*>(a.ws + cxa), z, __ATOMIC_RELAXED, __HIP_MEMORY_SCOPE_AGENT);
    }
    const float x = fx_val<4, 8192>(rs, t, WS_XC + (u32)(h * 2048 + tid) * 16, tag);
    const u32 xb = __float_as_uint(x), pa = dppb<0x124, 0xa>(dppb<0x12c, 0x5>(0u, xb), xb);
    const u32 pk = (c16 & 4) ? pk2(__uint_as_float(pa), x) : pk2(x, __uint_as_float(pa));
    const u32 alo = dppb<0x128, 0xc>(pk, pk), ahi = dppb<0x128, 0x3>(pk, pk);
    f32x4 s0 = f32x4{0.f, 0.f, 0.f, 0.f}, s1 = s0;
#pragma unroll
    for (int q = 0; q < 4; ++q) {
      const bool on = (c16 >> 2) == q;
      const s16x4 av = __builtin_bit_cast(s16x4, u32x2{on ? alo : 0u, on ? ahi : 0u});
      s0 = __builtin_amdgcn_mfma_f32_16x16x16bf16_1k(av, owf[2 * q], s0, 0, 0, 0);
      s1 = __builtin_amdgcn_mfma_f32_16x16x16bf16_1k(av, owf[2 * q + 1], s1, 0, 0, 0);
    }
#pragma unroll
    for (int b = 0; b < NB; ++b) *reinterpret_cast<LDSP u32x2*>((LDSP char*)exw + b * 512) = u32x2{__float_as_uint(s0[b]), __float_as_uint(s1[b])};
  }
  wg_barrier();
  {
    const float* pp = reinterpret_cast<const float*>(smem + L_EX) + tid;
    const float y = ((pp[0] + pp[512]) + (pp[1024] + pp[1536])) + ((pp[2048] + pp[2560]) + (pp[3072] + pp[3584]));
    const u64 t = fx_add(a.ws, rs, y, aoff, WS_XB + h * 32768 + word * 16, tag);
    if ((u32)(t >> 58) == 32u) { const float s = fx_val<32, 32768>(rs, t, WS_XB + word * 16, tag); *reinterpret_cast<u16*>(reinterpret_cast<char*>(a.out) + ooff) = (u16)pk2(s, s); }
  }
}

constexpr int upto(int ch) { const int y = PY + CU * (ch + 1), x = 4 * (ch + AW + 1), z = ch < 0 ? PY : (x > y ? x : y); return z < 4 * NCH ? z : 4 * NCH; }
DI void roleA(const KArgs& a, char* smem, const int g, const int idx, const int w, const int lane, const u32x4 (&R)[4], const u32 kvo, const u32 cpf, const u32 t0) {
  const int r = lane >> 4, c16 = lane & 15, tid = w * 64 + lane;
  const int h = 4 * (idx & 7) + ((idx >> 3) & 3), b = idx >> 5;
  const __amdgpu_buffer_rsrc_t rs = __builtin_amdgcn_make_buffer_rsrc((void*)a.ws, 0, (int)WS_END, RSRC_W3);
  u32 tag = *reinterpret_cast<const u32*>(a.ws + WS_EPOCH);
  u32x4 kv[16][4];
  u32x4 ov[16];
  const u16* wop = a.woa + (size_t)(h * DH + 64 * (c16 >> 3) + 8 * r) * DM + 64 * w + 8 * (c16 & 7);
  auto woload = [&](int n) INL { ov[n] = ldsh(wop + (size_t)(qoffs(n >> 2, b) + (n & 3)) * DM); };
  auto kvissue = [&](auto lo, auto hi) INL {
    unroll_c<hi - lo>([&](auto n0) INL {
      constexpr int n = lo + n0;
      constexpr int ch = n >> 2, isv = ch >> 3, st = ch & 7;
      if constexpr (n < PN2) pace<(n < PN1 ? PT0 + n * PD1 : PT0 + PN1 * PD1 + (n - PN1) * PD2)>(t0);
      kv[ch][n & 3] = ldnt(reinterpret_cast<const u16*>(reinterpret_cast<const char*>(isv ? a.vc : a.kc) + (size_t)(((st + (h >> 2)) & 7) * 4096) + 4 * (n & 3) * (DH * 2) + kvo));
      unroll_c<8>([&](auto k) INL { if constexpr (WK0 + k * WKD / 2 == n) { SCHED_FENCE(); woload(k); SCHED_FENCE(); } });
    });
  };
  {
    const int m0 = 128 * b + 16 * w + 4 * r;
    const u32x2 xf = *reinterpret_cast<const u32x2*>(a.xa + (c16 & 3) * DM + m0);
    SCHED_FENCE();
    __builtin_amdgcn_s_barrier();
    SCHED_FENCE();
    kvissue(IC(0), IC(A0));
    SCHED_FENCE();
    asm volatile("" :: "v"(cpf));
    PIN(tag); tag = next_tag(tag);
    f32x4 ac[8];
#pragma unroll
    for (int j = 0; j < 8; ++j) ac[j] = __builtin_amdgcn_mfma_f32_16x16x16bf16_1k(__builtin_bit_cast(s16x4, xf), kfrag(R, j), f32x4{0.f, 0.f, 0.f, 0.f}, 0, 0, 0);
    const f32x4 s0 = sel4(ac[0], ac[2], ac[4], ac[6], r), s1 = sel4(ac[1], ac[3], ac[5], ac[7], r);
    float* pp = reinterpret_cast<float*>(smem + L_AX) + w * 512 + ppos(r, c16);
#pragma unroll
    for (int k = 0; k < NB; ++k) *reinterpret_cast<u32x2*>(pp + k * DH) = u32x2{__float_as_uint(s0[k]), __float_as_uint(s1[k])};
  }
  wg_barrier();
  if (w < 4) {
    const float* pp = reinterpret_cast<const float*>(smem + L_AX) + 2 * tid;
    f32x2_ t[8];
#pragma unroll
    for (int k = 0; k < 8; ++k) t[k] = *reinterpret_cast<const f32x2_*>(pp + 512 * k);
    const f32x2_ s = ((t[0] + t[1]) + (t[2] + t[3])) + ((t[4] + t[5]) + (t[6] + t[7]));
    st_sc1(rs, WS_QX + (u32)(((h * 4 + w) * 4 + b) * DH + 2 * lane) * 8, u32x4{__float_as_uint(s[0]), tag, __float_as_uint(s[1]), tag});
  }
  SCHED_FENCE();
  __builtin_amdgcn_s_barrier();
  SCHED_FENCE();
  kvissue(IC(A0), IC(PX));
  SCHED_FENCE();
  u32x4 qpk;
  {
    const u32 off = WS_QX + (u32)(((h * 4 + b) * 4 + ((lane >> 3) & 3)) * DH + 16 * w + 2 * (lane & 7)) * 8;
    u32x4 v = ld_sc1(rs, off);
    SCHED_FENCE();
    kvissue(IC(PX), IC(PY));
    SCHED_FENCE();
    q_finish(rs, off, v, tag, smem + L_AQ + pcol(16 * w + 2 * (lane & 7)) * 2, lane);
    wg_barrier();
    qpk = *reinterpret_cast<const u32x4*>(smem + L_AQ + 16 * c16);
  }
  SCHED_FENCE();
  float sd[8];
  f32x4 acc[8];
  s16x4 pa[8];
  u32x4 wf[2][8];
  auto wpack = [&](int q) {
#pragma unroll
    for (int j = 0; j < 8; ++j) {
      const u32x2 t = __builtin_bit_cast(u32x2, kfrag(ov + 4 * q, j));
      wf[q >> 1][j][2 * (q & 1)] = t[0]; wf[q >> 1][j][2 * (q & 1) + 1] = t[1];
      PIN(wf[q >> 1][j]);
    }
  };
  const bool hi8 = c16 & 8, hi4 = c16 & 4;
  u32 aaf[4], aml = L_AST + 2304 + 8 * (lane & 7), asw = L_AST + w * 272 + 16 * c16;
#pragma unroll
  for (int q = 0; q < 4; ++q) { aaf[q] = L_AST + (c16 & 7) * 272 + (64 * (c16 >> 3) + 8 * r + qoffs(q, b)) * 2; PIN(aaf[q]); }
  u32 aoff = WS_ACC + b * 16384 + (tid >> 3) * 256 + (tid & 7) * 8, ooff = (b * 1024 + 64 * w + 8 * (c16 & 7) + 2 * r + (c16 >> 3)) * 2;
  PIN(aml); PIN(asw); PIN(aoff); PIN(ooff);
  const u32 foff = WS_FLG + (u32)(idx ^ (lane & 8)) * 128;
  u32x2 fl = u32x2{tag, tag};
  auto wit = [](int n) { return n < 8 ? -1 : n < 12 ? NCH - 1 : NCH; };
  auto wdn = [&](int n) { return (WK0 + n * WKD / 2) / 4 + 2; };
  auto steps = [&](auto lo, auto hi) INL {
  unroll_c<hi - lo>([&](auto ch0) INL {
    constexpr int ch = lo + ch0;
    SCHED_FENCE();
    kvissue(IC(upto(ch - 1)), IC(upto(ch)));
#pragma unroll
    for (int n = 0; n < 16; ++n)
      if (wit(n) == ch) woload(n);
    if (ch == FLI && w == FLW) fl = __builtin_amdgcn_raw_buffer_load_b64(rs, foff, 0, 16);
    SCHED_FENCE();
    if (ASYNC > 0 && ch % ASYNC == ASYNC - 1 && ch != 7 && ch < BVE) { __builtin_amdgcn_s_barrier(); SCHED_FENCE(); }
    if constexpr (ch < 7) {
      sd[ch] = score4(kv[ch], qpk, hi8, hi4);
      PIN(sd[ch]);
    } else if constexpr (ch == 7) {
      sd[7] = score4(kv[ch], qpk, hi8, hi4);
      float mn = fmaxf(fmaxf(fmaxf(sd[0], sd[1]), fmaxf(sd[2], sd[3])), fmaxf(fmaxf(sd[4], sd[5]), fmaxf(sd[6], sd[7])));
      mn = fmaxf(mn, dppf<0x128>(mn));
      mn = x32_max(x16_max(fmaxf(mn, dppf<0x124>(mn))));
#pragma unroll
      for (int k = 0; k < 8; ++k) sd[k] = __builtin_amdgcn_exp2f(sd[k] - mn);
      float l = ((sd[0] + sd[1]) + (sd[2] + sd[3])) + ((sd[4] + sd[5]) + (sd[6] + sd[7]));
      l += dppf<0x128>(l); l += dppf<0x124>(l);
      l = x32_sum(x16_sum(l));
      if (lane == 0) *reinterpret_cast<f32x2_*>(smem + L_AST + 2304 + 8 * w) = f32x2_{mn, l};
      wg_barrier();
      const f32x2_ ml = *reinterpret_cast<const LDSP f32x2_*>((const LDSP char*)aml);
      float M = fmaxf(ml[0], dppf<0xB1>(ml[0]));
      M = fmaxf(M, dppf<0x4E>(M)); M = fmaxf(M, dppf<0x141>(M));
      float L = __builtin_amdgcn_exp2f(ml[0] - M) * ml[1];
      L += dppf<0xB1>(L); L += dppf<0x4E>(L); L += dppf<0x141>(L);
      const float al = __builtin_amdgcn_exp2f(mn - M) * __builtin_amdgcn_rcpf(L);
#pragma unroll
      for (int k = 0; k < 8; ++k) { pa[k] = ppack(sd[k] * al); PIN(pa[k]); }
    } else {
#pragma unroll
      for (int j = 0; j < 8; ++j) acc[j] = __builtin_amdgcn_mfma_f32_16x16x16bf16_1k(pa[ch - 8], kfrag(kv[ch], j), ch == 8 ? f32x4{0.f, 0.f, 0.f, 0.f} : acc[j], 0, 0, 0);
#pragma unroll
      for (int j = 0; j < 8; ++j) PIN(acc[j]);
    }
#pragma unroll
    for (int q = 0; q < 2; ++q)
      if (wdn(4 * q + 3) - 1 == ch) wpack(q);
  });
  };
  steps(IC(0), IC(NCH - 1));
  if (__builtin_expect(!__all((fl[0] == tag) & (fl[1] == tag)), 0)) {
#pragma unroll 1
    for (u32 spins = 0; spins < SPIN_CAP; ++spins) {
      asm volatile("" ::: "memory");
      fl = __builtin_amdgcn_raw_buffer_load_b64(rs, foff, 0, 16);
      if (__all((fl[0] == tag) & (fl[1] == tag))) break;
      __builtin_amdgcn_s_sleep(2);
    }
  }
  steps(IC(NCH - 1), IC(NCH));
  if (lane < 16) *reinterpret_cast<LDSP u32x4*>((LDSP char*)asw) = u32x4{pk2(acc[0][0], acc[1][0]), pk2(acc[2][0], acc[3][0]), pk2(acc[4][0], acc[5][0]), pk2(acc[6][0], acc[7][0])};
  SCHED_FENCE();
#pragma unroll
  for (int n = 0; n < 16; ++n)
    if (wit(n) == NCH) woload(n);
  SCHED_FENCE();
  wg_barrier();
  {
    u32x2 af[4];
#pragma unroll
    for (int q = 0; q < 4; ++q) af[q] = *reinterpret_cast<const LDSP u32x2*>((const LDSP char*)aaf[q]);
    SCHED_FENCE();
    f32x4 D[8];
    auto JO = [](int j) { return (j >> 1) + 4 * (j & 1); };
#pragma unroll
    for (int j = 0; j < 8; ++j) {
      D[j] = mfma16(asbf(u32x4{af[0][0], af[0][1], af[1][0], af[1][1]}), asbf(wf[0][j]), f32x4{0.f, 0.f, 0.f, 0.f});
      SCHED_FENCE();
      wf[1][JO(j)] = __builtin_bit_cast(u32x2, kfrag(ov + 8, JO(j))).xyxy;
      PIN(wf[1][JO(j)][0]); PIN(wf[1][JO(j)][1]);
      SCHED_FENCE();
    }
#pragma unroll
    for (int j = 0; j < 8; ++j) {
      const u32x2 t = __builtin_bit_cast(u32x2, kfrag(ov + 12, JO(j)));
      wf[1][JO(j)][2] = t[0]; wf[1][JO(j)][3] = t[1];
      D[JO(j)] = mfma16(asbf(u32x4{af[2][0], af[2][1], af[3][0], af[3][1]}), asbf(wf[1][JO(j)]), D[JO(j)]);
      SCHED_FENCE();
    }
    float y[8], u[4], v[2];
#pragma unroll
    for (int j = 0; j < 8; ++j) { const f32x2_ t = __builtin_shufflevector(D[JO(j)], D[JO(j)], 0, 1) + __builtin_shufflevector(D[JO(j)], D[JO(j)], 2, 3); y[JO(j)] = t[0] + t[1]; }
#pragma unroll
    for (int k = 0; k < 4; ++k) { auto x = __builtin_amdgcn_permlane32_swap(__float_as_uint(y[k]), __float_as_uint(y[k + 4]), false, false); u[k] = __uint_as_float(hi8 ? x[1] : x[0]); }
#pragma unroll
    for (int k = 0; k < 2; ++k) { auto x = __builtin_amdgcn_permlane16_swap(__float_as_uint(u[k]), __float_as_uint(u[k + 2]), false, false); v[k] = __uint_as_float(x[0]) + __uint_as_float(x[1]); }
    const float yy = (hi8 ? v[1] : v[0]) + dppf<0x128>(hi8 ? v[0] : v[1]);
    const u64 t = fx_add(a.ws, rs, yy, aoff, WS_XA + h * 32768 + (b * 512 + tid) * 16, tag);
    if ((u32)(t >> 58) == 32u) { const float s = fx_val<32, 32768>(rs, t, WS_XA + (b * 512 + tid) * 16, tag); *reinterpret_cast<u16*>(reinterpret_cast<char*>(a.out) + ooff) = (u16)pk2(s, s); }
  }
  asm volatile("s_endpgm" ::: "memory");
}

__global__ void __launch_bounds__(512, 2) fused(KArgs a) {
  extern __shared__ __attribute__((aligned(16))) char smem[];
  const int tid = threadIdx.x, lane = tid & 63, w = __builtin_amdgcn_readfirstlane(tid >> 6), g = blockIdx.x;
  const int idx = (g & 7) | ((g >> 4) << 3);
  const u64 traw = __builtin_amdgcn_s_memrealtime();
  const u32 t0 = (u32)traw;
  u32 cpf = 0;
  if (w >= 8 - CPF && (idx >> 3) < CPN) {
    u64 pc; asm volatile("s_getpc_b64 %0" : "=s"(pc));
    cpf = *reinterpret_cast<const __attribute__((address_space(1))) u32*>((pc & ~127ull) + min((7 - w) * 64 + lane, CPL) * 128);
  }
  SCHED_FENCE();
  u32 o[4], o2;
  {
    const u32 h = 4 * (idx & 7) + ((idx >> 3) & 3), b = idx >> 5, r = lane >> 4;
#pragma unroll
    for (int n = 0; n < 4; ++n) { o[n] = (128 * b + 16 * w + 4 * r + n) * (NH * DH * 2) + h * (DH * 2) + (lane & 15) * 16; PIN(o[n]); }
    u32 ob = (((idx & 3) * SK + ((idx >> 4) + (idx & 8) + 4 * (idx & 4)) * 128 + w) * DC + 8 * lane) * 2, oa = (((b * NH + h) * SK + (SK - WIN) + 128 * w + r) * DH + 8 * (lane & 15)) * 2;
    PIN(oa); PIN(ob);
    o2 = ((g >> 3) & 1) ? ob : oa;
    PIN(o2);
  }
  SCHED_FENCE();
  asm volatile("" :: "s"(a.xa), "s"(a.kc), "s"(a.vc), "s"(a.wqa), "s"(a.woa), "s"(a.xb), "s"(a.ckv), "s"(a.wqb), "s"(a.wuk), "s"(a.wuv), "s"(a.wob), "s"(a.ws), "s"(a.out), "s"(traw));
  u32x4 R[4];
  {
    const char* p = reinterpret_cast<const char*>(((g >> 3) & 1) ? a.wuk : a.wqa);
#pragma unroll
    for (int n = 0; n < 4; ++n) R[n] = ldnt(reinterpret_cast<const u16*>(p + o[n]));
    SCHED_FENCE();
  }
  if (__builtin_expect(!((g >> 3) & 1), 1)) roleA(a, smem, g, idx, w, lane, R, o2, cpf, t0);
  else roleB(a, smem, g, idx, w, lane, R, o2, cpf, t0);
}

extern "C" void kernel_launch(void* const* d_in, const int* in_sizes, int n_in,
                              void* d_out, int out_size, void* d_ws, size_t ws_size,
                              hipStream_t stream) {
  KArgs a;
  a.xa  = (const u16*)d_in[0];
  a.kc  = (const u16*)d_in[1];
  a.vc  = (const u16*)d_in[2];
  a.wqa = (const u16*)d_in[3];
  a.woa = (const u16*)d_in[4];
  a.xb  = (const u16*)d_in[5];
  a.ckv = (const u16*)d_in[6];
  a.wqb = (const u16*)d_in[7];
  a.wuk = (const u16*)d_in[8];
  a.wuv = (const u16*)d_in[9];
  a.wob = (const u16*)d_in[10];
  {
    const size_t need = (size_t)WS_END + (1u << 20);
    const size_t off = ws_size > need ? (((ws_size - need) / 8 * WOFF) & ~(size_t)0x1fffff) : 0;
    a.ws = (char*)d_ws + off;
  }
  a.out = (u16*)d_out;
  (void)hipFuncSetAttribute((const void*)fused, hipFuncAttributeMaxDynamicSharedMemorySize, L_END);
  fused<<<256, 512, L_END, stream>>>(a);
}
```
